```python
import torch

def _softmax(x, axis=-1):
    return torch.softmax(x, dim=axis)
def _gelu(x):
    return torch.nn.functional.gelu(x, approximate="tanh")
def _silu(x):
    return torch.nn.functional.silu(x)
def _sigmoid(x):
    return torch.sigmoid(x)
def _layernorm(x, axis=-1, eps=1e-5):
    m = x.mean(dim=axis, keepdim=True)
    v = x.var(dim=axis, keepdim=True, unbiased=False)
    return (x - m) / torch.sqrt(v + eps)
def _scatter_add(tgt, idx, src, axis=0):
    return tgt.clone().index_add_(axis, idx, src)
def _segment_sum(src, seg, n):
    out = torch.zeros((n,) + src.shape[1:], dtype=src.dtype)
    return out.index_add_(0, seg, src)
def _pad(x, pw):
    flat = tuple(v for pair in reversed(pw) for v in pair)
    return torch.nn.functional.pad(x, flat)

kernel_name = 'standalone__attention_interact_autoint__recsys'

def setup_inputs():

    _w = (lambda a: (lambda t: t.to(torch.bfloat16) if t.is_floating_point() else t)(torch.as_tensor(a)))
    B, F = 4096, 100
    D = max(2, (64 // 2) * 2)
    H = 2
    Dh = D // H
    return {
        "e": _w((lambda *s: torch.randn(*s, dtype=torch.bfloat16) * 0.05)(B, F, D).to(float)),
        "wq": _w((lambda *s: torch.randn(*s, dtype=torch.bfloat16) * 3.2)(H, D, Dh).to(float)),
        "wk": _w((lambda *s: torch.randn(*s, dtype=torch.bfloat16) * 3.2)(H, D, Dh).to(float)),
        "wv": _w((lambda *s: torch.randn(*s, dtype=torch.bfloat16) * 0.05)(H, D, Dh).to(float)),
        "wres": _w((lambda *s: torch.randn(*s, dtype=torch.bfloat16) * 0.05)(D, D).to(float)),
    }

def reference(e, wq, wk, wv, wres):
    H, D, Dh = wq.shape[0], wq.shape[1], wq.shape[2]
    B, F = e.shape[0], e.shape[1]
    q = torch.einsum('bfd,hdk->bhfk', e, wq)
    k = torch.einsum('bfd,hdk->bhfk', e, wk)
    v = torch.einsum('bfd,hdk->bhfk', e, wv)
    scale = float(Dh) ** -0.5
    scores = torch.einsum('bhfk,bhgk->bhfg', q, k) * scale
    attn = _softmax(scores, axis=-1)
    attn = (lambda x, dt: x.to(dt))(attn, v.dtype)
    ctx = torch.einsum('bhfg,bhgk->bhfk', attn, v)
    ctx = (lambda x, *d: x.permute(*d))(ctx, 0, 2, 1, 3).reshape(B, F, H * Dh)
    res = torch.einsum('bfd,de->bfe', e, wres)
    out = ctx + res
    mask = (lambda x, dt: x.to(dt))(out > 0, out.dtype)
    return out * mask
```

```cpp
#include <hip/hip_runtime.h>
#include <hip/hip_bf16.h>

typedef __attribute__((ext_vector_type(8))) __bf16 bf16x8;
typedef __attribute__((ext_vector_type(4))) short s16x4;
typedef __attribute__((ext_vector_type(2))) __bf16 bf16x2;
typedef __attribute__((ext_vector_type(2))) float f32x2;
typedef __attribute__((ext_vector_type(4))) float f32x4;
typedef __attribute__((ext_vector_type(4))) unsigned u32x4;
typedef __attribute__((ext_vector_type(2))) unsigned u32x2;
typedef __attribute__((ext_vector_type(2))) short s16x2;
#define LDSP(T) __attribute__((address_space(3))) T*

constexpr int NF = 100, ND = 64;
constexpr int ROWB = 128;
constexpr int BATB = NF * ROWB;
constexpr int EBUF = 96 * ROWB;
constexpr int PSTR = 320;
constexpr int PSCR = 5 * PSTR;
constexpr int LDS_BYTES = 2 * EBUF + 2 * PSCR;
constexpr float QSCALE = 0.17677669529663687f * 1.4426950408889634f;
constexpr unsigned BF_VS = 0x3380u;
constexpr unsigned BF_ONE = 0x3f80u;

__device__ __forceinline__ f32x4 mfma32(u32x4 a, u32x4 b, f32x4 c) {
    return __builtin_amdgcn_mfma_f32_16x16x32_bf16(__builtin_bit_cast(bf16x8, a), __builtin_bit_cast(bf16x8, b), c, 0, 0, 0);
}
__device__ __forceinline__ f32x4 mfma16(u32x2 a, u32x2 b, f32x4 c) {
    return __builtin_amdgcn_mfma_f32_16x16x16bf16_1k(__builtin_bit_cast(s16x4, a), __builtin_bit_cast(s16x4, b), c, 0, 0, 0);
}
__device__ __forceinline__ unsigned pk(float lo, float hi) {
    f32x2 v = {lo, hi};
    return __builtin_bit_cast(unsigned, __builtin_convertvector(v, bf16x2));
}
__device__ __forceinline__ u32x4 pack8(f32x4 a, f32x4 b) { return (u32x4){pk(a[0], a[1]), pk(a[2], a[3]), pk(b[0], b[1]), pk(b[2], b[3])}; }
__device__ __forceinline__ u32x4 packi(f32x4 a, f32x4 b) { return (u32x4){pk(a[0], b[0]), pk(a[1], b[1]), pk(a[2], b[2]), pk(a[3], b[3])}; }
__device__ __forceinline__ unsigned relu2(unsigned x) {
    s16x2 v = __builtin_bit_cast(s16x2, x); s16x2 z = {0, 0};
    return __builtin_bit_cast(unsigned, __builtin_elementwise_max(v, z));
}
__device__ __forceinline__ float ex2(float x) { return __builtin_amdgcn_exp2f(x); }
__device__ __forceinline__ void glds16(const void* g, unsigned lds_base) {
    unsigned sv;
    asm volatile("s_mov_b32 %0, m0\n\ts_mov_b32 m0, %2\n\ts_nop 0\n\tglobal_load_lds_dwordx4 %1, off\n\ts_mov_b32 m0, %0"
                 : "=&s"(sv) : "v"(g), "s"(lds_base) : "memory");
}
constexpr f32x4 Z4 = {0.f, 0.f, 0.f, 0.f};

struct Wfrag { u32x4 q[2][2], k[2][2], v[2][2], r[2][2]; };

__device__ __forceinline__ void load_w(u32x4 (&w)[2][2], const char* m, int rstride, int c, int g, u32x2 ident, float post) {
#pragma unroll
    for (int s = 0; s < 2; ++s) {
        u32x4 raw[2];
#pragma unroll
        for (int t = 0; t < 2; ++t) raw[t] = *(const u32x4*)(m + (32 * s + 8 * (c >> 2) + 4 * t + (c & 3)) * rstride + 16 * g);
#pragma unroll
        for (int dt = 0; dt < 2; ++dt) {
            f32x4 d0 = mfma16((u32x2){raw[0][2 * dt], raw[0][2 * dt + 1]}, ident, Z4);
            f32x4 d1 = mfma16((u32x2){raw[1][2 * dt], raw[1][2 * dt + 1]}, ident, Z4);
            if (post != 1.0f) { d0 *= post; d1 *= post; }
            w[dt][s] = pack8(d0, d1);
        }
    }
}

__device__ __forceinline__ void slow_unit(const Wfrag& W, const char* eb, char* ob, int c, int g) {
    u32x4 K[7], V[4][2];
    f32x4 va[2][2];
#pragma unroll
    for (int t = 0; t < 8; ++t) {
        if (t < 7) {
            const int row = min(16 * t + c, NF - 1);
            u32x4 e0 = *(const u32x4*)(eb + row * ROWB + 16 * g), e1 = *(const u32x4*)(eb + row * ROWB + 64 + 16 * g);
            K[t] = pack8(mfma32(W.k[0][1], e1, mfma32(W.k[0][0], e0, Z4)), mfma32(W.k[1][1], e1, mfma32(W.k[1][0], e0, Z4)));
            va[t & 1][0] = mfma32(e1, W.v[0][1], mfma32(e0, W.v[0][0], Z4));
            va[t & 1][1] = mfma32(e1, W.v[1][1], mfma32(e0, W.v[1][0], Z4));
        } else { va[1][0] = Z4; va[1][1] = Z4; }
        if (t & 1) { V[t >> 1][0] = packi(va[0][0], va[1][0]); V[t >> 1][1] = packi(va[0][1], va[1][1]); }
    }
    const u32x4 ones = {BF_VS * 0x10001u, BF_VS * 0x10001u, BF_VS * 0x10001u, BF_VS * 0x10001u};
#pragma unroll 1
    for (int t = 0; t < 7; ++t) {
        const int row = min(16 * t + c, NF - 1);
        u32x4 e0 = *(const u32x4*)(eb + row * ROWB + 16 * g), e1 = *(const u32x4*)(eb + row * ROWB + 64 + 16 * g);
        u32x4 Q = pack8(mfma32(W.q[0][1], e1, mfma32(W.q[0][0], e0, Z4)), mfma32(W.q[1][1], e1, mfma32(W.q[1][0], e0, Z4)));
        f32x4 r0 = mfma32(W.r[0][1], e1, mfma32(W.r[0][0], e0, Z4)), r1 = mfma32(W.r[1][1], e1, mfma32(W.r[1][0], e0, Z4));
        f32x4 S[8];
        float m = -INFINITY;
#pragma unroll
        for (int kt = 0; kt < 7; ++kt) {
            S[kt] = mfma32(K[kt], Q, Z4);
            if (kt == 6 && g != 0) S[kt] = (f32x4){-INFINITY, -INFINITY, -INFINITY, -INFINITY};
#pragma unroll
            for (int r = 0; r < 4; ++r) m = fmaxf(m, S[kt][r]);
        }
        m = fmaxf(m, __shfl_xor(m, 16)); m = fmaxf(m, __shfl_xor(m, 32));
#pragma unroll
        for (int kt = 0; kt < 7; ++kt)
#pragma unroll
            for (int r = 0; r < 4; ++r) S[kt][r] = ex2(S[kt][r] - m);
        S[7] = Z4;
        f32x4 o0 = Z4, o1 = Z4, l = Z4;
#pragma unroll
        for (int u = 0; u < 4; ++u) {
            u32x4 P = packi(S[2 * u], S[2 * u + 1]);
            o0 = mfma32(V[u][0], P, o0); o1 = mfma32(V[u][1], P, o1); l = mfma32(ones, P, l);
        }
        const float rl = 1.0f / l[0];
        u32x4 o = pack8(o0 * rl + r0, o1 * rl + r1);
        o = (u32x4){relu2(o[0]), relu2(o[1]), relu2(o[2]), relu2(o[3])};
        if (16 * t + c < NF) *(u32x4*)(ob + row * ROWB + 16 * g) = o;
    }
}

__global__ void __launch_bounds__(128, 2)
autoint_kernel(const char* __restrict__ e, const char* __restrict__ wq, const char* __restrict__ wk, const char* __restrict__ wv,
               const char* __restrict__ wres, char* __restrict__ out, int nb, int nwg) {
    __shared__ __attribute__((aligned(16))) char lds[LDS_BYTES];
    const int lane = threadIdx.x & 63, c = lane & 15, g = lane >> 4;
    const int h = __builtin_amdgcn_readfirstlane(threadIdx.x >> 6);
    const int w = blockIdx.x;
    const unsigned lds0 = (unsigned)(uintptr_t)lds;
    auto bat = [&](int u) { return min(w + u * nwg, nb - 1); };

    const int dsrc = h * 1024 + (lane >> 3) * ROWB + 16 * ((lane & 7) ^ ((4 * h + (lane >> 4)) & 7));
    auto dma = [&](int u) {
        const char* src = e + (size_t)bat(u) * BATB + dsrc;
        const unsigned dst = lds0 + (u & 1) * EBUF + h * 1024;
#pragma unroll
        for (int i = 0; i < 6; ++i) glds16(src + i * 2048, dst + i * 2048);
    };
    dma(0);

    Wfrag W;
    {
        const unsigned one = (g == (c >> 2)) ? ((c & 1) ? 0x10000u : 1u) : 0u;
        const u32x2 id = {(c & 2) ? 0u : one, (c & 2) ? one : 0u};
        load_w(W.k, wk + h * 4096, 64, c, g, id * BF_ONE, 1.0f);
        load_w(W.v, wv + h * 4096, 64, c, g, id * BF_VS, 1.0f);
        load_w(W.q, wq + h * 4096, 64, c, g, id * BF_ONE, QSCALE);
        load_w(W.r, wres + h * 64, 128, c, g, id * BF_ONE, 1.0f);
    }
    u32x4 Qr, Kr; u32x2 Vr[2]; f32x4 rr[2];
    const size_t remoff = (size_t)bat(c & 3) * BATB + (96 + (c >> 2)) * ROWB + 16 * g;
    {
        u32x4 e0 = *(const u32x4*)(e + remoff), e1 = *(const u32x4*)(e + remoff + 64);
        Qr = pack8(mfma32(W.q[0][1], e1, mfma32(W.q[0][0], e0, Z4)), mfma32(W.q[1][1], e1, mfma32(W.q[1][0], e0, Z4)));
        Kr = pack8(mfma32(W.k[0][1], e1, mfma32(W.k[0][0], e0, Z4)), mfma32(W.k[1][1], e1, mfma32(W.k[1][0], e0, Z4)));
#pragma unroll
        for (int dt = 0; dt < 2; ++dt) {
            f32x4 v = mfma32(e1, W.v[dt][1], mfma32(e0, W.v[dt][0], Z4));
            Vr[dt] = (u32x2){pk(v[0], v[1]), pk(v[2], v[3])};
            rr[dt] = mfma32(W.r[dt][1], e1, mfma32(W.r[dt][0], e0, Z4));
        }
    }
    LDSP(char) ps = (LDSP(char))lds + 2 * EBUF + h * PSCR;
    *(LDSP(unsigned))(ps + 4 * PSTR + 4 * lane) = 0u;
    const u32x4 ones = {BF_VS * 0x10001u, BF_VS * 0x10001u, BF_VS * 0x10001u, BF_VS * 0x10001u};
    const u32x2 one1 = {BF_VS, 0u};
    f32x4 or0 = Z4, or1 = Z4, lr = Z4;
    unsigned flags = 0;
    const int erd = c * ROWB + 16 * (g ^ ((c >> 1) & 7));
    constexpr unsigned LO = 0x2b800000u  , HI = 0x43000000u  ;

#pragma unroll 1
    for (int u = 0; u < 4; ++u) {
        if (u == 0) asm volatile("s_waitcnt vmcnt(0) lgkmcnt(0)\n\ts_barrier" ::: "memory");
        else asm volatile("s_waitcnt vmcnt(6) lgkmcnt(0)\n\ts_barrier" ::: "memory");
        if (u < 3) dma(u + 1);
        LDSP(const char) eb = (LDSP(const char))lds + (u & 1) * EBUF;
        char* ob = out + (size_t)bat(u) * BATB + h * 64 + c * ROWB + 16 * g;
        u32x4 K[6], V[3][2];
        f32x4 va[2][2];
#pragma unroll
        for (int t = 0; t < 6; ++t) {
            u32x4 e0 = *(LDSP(const u32x4))(eb + t * 2048 + erd), e1 = *(LDSP(const u32x4))(eb + t * 2048 + (erd ^ 64));
            K[t] = pack8(mfma32(W.k[0][1], e1, mfma32(W.k[0][0], e0, Z4)), mfma32(W.k[1][1], e1, mfma32(W.k[1][0], e0, Z4)));
            va[t & 1][0] = mfma32(e1, W.v[0][1], mfma32(e0, W.v[0][0], Z4));
            va[t & 1][1] = mfma32(e1, W.v[1][1], mfma32(e0, W.v[1][0], Z4));
            if (t & 1) { V[t >> 1][0] = packi(va[0][0], va[1][0]); V[t >> 1][1] = packi(va[0][1], va[1][1]); }
        }
        const u32x2 Vc[2] = {{Vr[0][0] & 0xffffu, 0u}, {Vr[1][0] & 0xffffu, 0u}};
        {
#pragma unroll
            for (int k2 = 0; k2 < 3; ++k2) {
                const float a = ex2(mfma32(Qr, K[2 * k2], Z4)[0]), b = ex2(mfma32(Qr, K[2 * k2 + 1], Z4)[0]);
                *(LDSP(unsigned))(ps + g * PSTR + 64 * k2 + 4 * c) = pk(a, b);
            }
            float a = ex2(mfma32(Qr, Kr, Z4)[0]);
            a = (c & 3) ? 0.f : a;
            *(LDSP(unsigned short))(ps + g * PSTR + 192 + 2 * c) = (unsigned short)pk(a, a);
            LDSP(const char) pr = ps + (((c & 3) == u) ? (c >> 2) : 4) * PSTR;
#pragma unroll
            for (int k2 = 0; k2 < 3; ++k2) {
                const u32x4 P = *(LDSP(const u32x4))(pr + 64 * k2 + 16 * g);
                or0 = mfma32(V[k2][0], P, or0); or1 = mfma32(V[k2][1], P, or1); lr = mfma32(ones, P, lr);
            }
            const u32x2 P = *(LDSP(const u32x2))(pr + 192 + 8 * g);
            or0 = mfma16(Vc[0], P, or0); or1 = mfma16(Vc[1], P, or1); lr = mfma16(one1, P, lr);
        }
        unsigned bad = 0;
#pragma unroll
        for (int t = 0; t < 6; ++t) {
            u32x4 e0 = *(LDSP(const u32x4))(eb + t * 2048 + erd), e1 = *(LDSP(const u32x4))(eb + t * 2048 + (erd ^ 64));
            const u32x4 Q = pack8(mfma32(W.q[0][1], e1, mfma32(W.q[0][0], e0, Z4)), mfma32(W.q[1][1], e1, mfma32(W.q[1][0], e0, Z4)));
            const f32x4 r0 = mfma32(W.r[0][1], e1, mfma32(W.r[0][0], e0, Z4)), r1 = mfma32(W.r[1][1], e1, mfma32(W.r[1][0], e0, Z4));
            f32x4 o0 = Z4, o1 = Z4, l = Z4;
#pragma unroll
            for (int k2 = 0; k2 < 3; ++k2) {
                f32x4 a = mfma32(K[2 * k2], Q, Z4), b = mfma32(K[2 * k2 + 1], Q, Z4);
#pragma unroll
                for (int r = 0; r < 4; ++r) { a[r] = ex2(a[r]); b[r] = ex2(b[r]); }
                const u32x4 P = packi(a, b);
                o0 = mfma32(V[k2][0], P, o0); o1 = mfma32(V[k2][1], P, o1); l = mfma32(ones, P, l);
            }
            {
                const float a = ex2(mfma32(Kr, Q, Z4)[0]);
                const u32x2 P = {pk(a, a), 0u};
                o0 = mfma16(Vc[0], P, o0); o1 = mfma16(Vc[1], P, o1); l = mfma16(one1, P, l);
            }
            bad = max(bad, __float_as_uint(l[0]) - LO);
            const float rl = __builtin_amdgcn_rcpf(l[0]);
            f32x4 x0, x1;
#pragma unroll
            for (int r = 0; r < 4; ++r) { x0[r] = __builtin_fmaf(o0[r], rl, r0[r]); x1[r] = __builtin_fmaf(o1[r], rl, r1[r]); }
            u32x4 o = pack8(x0, x1);
            o = (u32x4){relu2(o[0]), relu2(o[1]), relu2(o[2]), relu2(o[3])};
            *(u32x4*)(ob + t * 2048) = o;
        }
        if (__any(bad > HI - LO)) flags |= 1u << u;
#pragma unroll
        for (int i = 0; i < 4; ++i) {
            Qr[i] = __builtin_amdgcn_mov_dpp(Qr[i], 0x39, 0xf, 0xf, false);
            Kr[i] = __builtin_amdgcn_mov_dpp(Kr[i], 0x39, 0xf, 0xf, false);
        }
#pragma unroll
        for (int dt = 0; dt < 2; ++dt) { Vr[dt][0] = __builtin_amdgcn_alignbit(Vr[dt][1], Vr[dt][0], 16); Vr[dt][1] >>= 16; }
    }
    {
        const bool bad = (__float_as_uint(lr[0]) - LO) > HI - LO;
        const unsigned long long bm = __ballot(bad);
#pragma unroll
        for (int u = 0; u < 4; ++u) if (bm & (0x1111ull << u)) flags |= 1u << u;
        const float rl = __builtin_amdgcn_rcpf(lr[0]);
        u32x4 o = pack8(or0 * rl + rr[0], or1 * rl + rr[1]);
        o = (u32x4){relu2(o[0]), relu2(o[1]), relu2(o[2]), relu2(o[3])};
        *(u32x4*)(out + remoff + h * 64) = o;
    }
    if (__builtin_expect(flags != 0, 0)) {
        asm volatile("s_waitcnt vmcnt(0)" ::: "memory");
#pragma unroll 1
        for (int u = 0; u < 4; ++u)
            if (flags & (1u << u)) slow_unit(W, e + (size_t)bat(u) * BATB, out + (size_t)bat(u) * BATB + h * 64, c, g);
    }
}

extern "C" void kernel_launch(void* const* d_in, const int* in_sizes, int n_in, void* d_out, int out_size, void* d_ws, size_t ws_size,
                              hipStream_t stream) {
    const int nb = in_sizes[0] / (NF * ND);
    const int nwg = (nb + 3) / 4;
    autoint_kernel<<<nwg, 128, 0, stream>>>((const char*)d_in[0], (const char*)d_in[1], (const char*)d_in[2], (const char*)d_in[3],
                                            (const char*)d_in[4], (char*)d_out, nb, nwg);
}
```

```cpp
#include <hip/hip_runtime.h>
#include <hip/hip_bf16.h>

typedef __attribute__((ext_vector_type(8))) __bf16 bf16x8;
typedef __attribute__((ext_vector_type(4))) short s16x4;
typedef __attribute__((ext_vector_type(2))) __bf16 bf16x2;
typedef __attribute__((ext_vector_type(2))) float f32x2;
typedef __attribute__((ext_vector_type(4))) float f32x4;
typedef __attribute__((ext_vector_type(4))) unsigned u32x4;
typedef __attribute__((ext_vector_type(2))) unsigned u32x2;
typedef __attribute__((ext_vector_type(2))) short s16x2;

constexpr int NF = 100, ND = 64;
constexpr int ROWB = 128;
constexpr int BATB = NF * ROWB;
constexpr int BUFSZ = 4 * 12288;
constexpr int LDS_BYTES = 3 * BUFSZ + 8 * 1600;
constexpr float QSCALE = 0.17677669529663687f * 1.4426950408889634f;
constexpr unsigned BF_ONE = 0x3f80u;

__device__ __forceinline__ f32x4 mfma32(u32x4 a, u32x4 b, f32x4 c) {
    return __builtin_amdgcn_mfma_f32_16x16x32_bf16(__builtin_bit_cast(bf16x8, a), __builtin_bit_cast(bf16x8, b), c, 0, 0, 0);
}
__device__ __forceinline__ f32x4 mfma16(u32x2 a, u32x2 b, f32x4 c) {
    return __builtin_amdgcn_mfma_f32_16x16x16bf16_1k(__builtin_bit_cast(s16x4, a), __builtin_bit_cast(s16x4, b), c, 0, 0, 0);
}
__device__ __forceinline__ unsigned pk(float lo, float hi) {
    f32x2 v = {lo, hi};
    return __builtin_bit_cast(unsigned, __builtin_convertvector(v, bf16x2));
}
__device__ __forceinline__ u32x4 pack8(f32x4 a, f32x4 b) { return (u32x4){pk(a[0], a[1]), pk(a[2], a[3]), pk(b[0], b[1]), pk(b[2], b[3])}; }
__device__ __forceinline__ u32x4 packi(f32x4 a, f32x4 b) { return (u32x4){pk(a[0], b[0]), pk(a[1], b[1]), pk(a[2], b[2]), pk(a[3], b[3])}; }
__device__ __forceinline__ unsigned relu2(unsigned x) {
    s16x2 v = __builtin_bit_cast(s16x2, x); s16x2 z = {0, 0};
    return __builtin_bit_cast(unsigned, __builtin_elementwise_max(v, z));
}
constexpr f32x4 Z4 = {0.f, 0.f, 0.f, 0.f};
struct Wfrag { u32x4 q[2][2], k[2][2], v[2][2], r[2][2]; };

__device__ __forceinline__ void load_w(u32x4 (&w)[2][2], const char* m, int rstride, int c, int g, u32x2 ident, float post) {
#pragma unroll
    for (int s = 0; s < 2; ++s) {
        u32x4 raw[2];
#pragma unroll
        for (int t = 0; t < 2; ++t) raw[t] = *(const u32x4*)(m + (32 * s + 8 * (c >> 2) + 4 * t + (c & 3)) * rstride + 16 * g);
#pragma unroll
        for (int dt = 0; dt < 2; ++dt) {
            f32x4 d0 = mfma16((u32x2){raw[0][2 * dt], raw[0][2 * dt + 1]}, ident, Z4);
            f32x4 d1 = mfma16((u32x2){raw[1][2 * dt], raw[1][2 * dt + 1]}, ident, Z4);
            if (post != 1.0f) { d0 *= post; d1 *= post; }
            w[dt][s] = pack8(d0, d1);
        }
    }
}
__device__ __forceinline__ void load_all_w(Wfrag& W, const char* wq, const char* wk, const char* wv, const char* wres, int h, int c, int g) {
    const unsigned one = (g == (c >> 2)) ? ((c & 1) ? 0x10000u : 1u) : 0u;
    const u32x2 id = {(c & 2) ? 0u : one * BF_ONE, (c & 2) ? one * BF_ONE : 0u};
    load_w(W.k, wk + h * 4096, 64, c, g, id, 1.0f);
    load_w(W.v, wv + h * 4096, 64, c, g, id, 1.0f);
    load_w(W.q, wq + h * 4096, 64, c, g, id, QSCALE);
    load_w(W.r, wres + h * 64, 128, c, g, id, 1.0f);
}

__device__ __forceinline__ void slow_unit(const Wfrag& W, const char* eb, char* ob, int c, int g) {
    u32x4 K[7], V[4][2];
    f32x4 va[2][2];
#pragma unroll
    for (int t = 0; t < 8; ++t) {
        if (t < 7) {
            const int row = min(16 * t + c, NF - 1);
            u32x4 e0 = *(const u32x4*)(eb + row * ROWB + 16 * g), e1 = *(const u32x4*)(eb + row * ROWB + 64 + 16 * g);
            K[t] = pack8(mfma32(W.k[0][1], e1, mfma32(W.k[0][0], e0, Z4)), mfma32(W.k[1][1], e1, mfma32(W.k[1][0], e0, Z4)));
            va[t & 1][0] = mfma32(e1, W.v[0][1], mfma32(e0, W.v[0][0], Z4));
            va[t & 1][1] = mfma32(e1, W.v[1][1], mfma32(e0, W.v[1][0], Z4));
        } else { va[1][0] = Z4; va[1][1] = Z4; }
        if (t & 1) { V[t >> 1][0] = packi(va[0][0], va[1][0]); V[t >> 1][1] = packi(va[0][1], va[1][1]); }
    }
    const u32x4 ones = {BF_ONE * 0x10001u, BF_ONE * 0x10001u, BF_ONE * 0x10001u, BF_ONE * 0x10001u};
#pragma unroll 1
    for (int t = 0; t < 7; ++t) {
        const int row = min(16 * t + c, NF - 1);
        u32x4 e0 = *(const u32x4*)(eb + row * ROWB + 16 * g), e1 = *(const u32x4*)(eb + row * ROWB + 64 + 16 * g);
        u32x4 Q = pack8(mfma32(W.q[0][1], e1, mfma32(W.q[0][0], e0, Z4)), mfma32(W.q[1][1], e1, mfma32(W.q[1][0], e0, Z4)));
        f32x4 r0 = mfma32(W.r[0][1], e1, mfma32(W.r[0][0], e0, Z4)), r1 = mfma32(W.r[1][1], e1, mfma32(W.r[1][0], e0, Z4));
        f32x4 S[8];
        float m = -INFINITY;
#pragma unroll
        for (int kt = 0; kt < 7; ++kt) {
            S[kt] = mfma32(K[kt], Q, Z4);
            if (kt == 6 && g != 0) S[kt] = (f32x4){-INFINITY, -INFINITY, -INFINITY, -INFINITY};
#pragma unroll
            for (int r = 0; r < 4; ++r) m = fmaxf(m, S[kt][r]);
        }
        m = fmaxf(m, __shfl_xor(m, 16)); m = fmaxf(m, __shfl_xor(m, 32));
#pragma unroll
        for (int kt = 0; kt < 7; ++kt)
#pragma unroll
            for (int r = 0; r < 4; ++r) S[kt][r] = __builtin_amdgcn_exp2f(S[kt][r] - m);
        S[7] = Z4;
        f32x4 o0 = Z4, o1 = Z4, l = Z4;
#pragma unroll
        for (int u = 0; u < 4; ++u) {
            u32x4 P = packi(S[2 * u], S[2 * u + 1]);
            o0 = mfma32(V[u][0], P, o0); o1 = mfma32(V[u][1], P, o1); l = mfma32(ones, P, l);
        }
        const float rl = 1.0f / l[0];
        u32x4 o = pack8(o0 * rl + r0, o1 * rl + r1);
        o = (u32x4){relu2(o[0]), relu2(o[1]), relu2(o[2]), relu2(o[3])};
        if (16 * t + c < NF) *(u32x4*)(ob + row * ROWB + 16 * g) = o;
    }
}

__global__ void __launch_bounds__(512, 2)
autoint_kernel(const char* __restrict__ e, char* __restrict__ out, unsigned stride, unsigned vscale, const char* __restrict__ wk,
               const char* __restrict__ wv, const char* __restrict__ wq, const char* __restrict__ wres) {
    __shared__ __attribute__((aligned(16))) char lds[LDS_BYTES];
    const int j = __builtin_amdgcn_readfirstlane(threadIdx.x >> 6);
    unsigned flag;
    asm volatile(
        "s_lshr_b32 s34, %[j], 1\n"
        "s_load_dwordx4 s[40:43], %[ka], 0x0\n"
        "s_lshl_b32 s56, s34, 3\n"
        "s_add_u32 s56, s56, 24\n"
        "s_load_dwordx2 s[64:65], %[ka], s56\n"
        "s_load_dwordx2 s[36:37], %[ka], 0x10\n"
        "s_mov_b32 s68, m0\n"
        "s_and_b32 s35, %[j], 1\n"
        "s_cmp_eq_u32 s34, 3\n"
        "s_cselect_b32 s57, 7, 6\n"
        "s_cselect_b32 s58, 6, 12\n"
        "s_lshl_b32 s58, s35, s58\n"
        "s_lshl_b32 s59, %[w], 2\n"
        "s_add_u32 s59, s59, s34\n"
        "s_mul_hi_u32 s61, s59, 12800\n"
        "s_mul_i32 s60, s59, 12800\n"
        "s_lshl_b32 s56, s35, 2\n"
        "s_lshl_b32 s62, s35, 10\n"
        "s_mul_i32 s63, s34, 12288\n"
        "s_add_u32 s63, s63, %[lds]\n"
        "s_add_u32 s50, s63, s62\n"
        "s_lshl_b32 s66, %[j], 11\n"
        "s_add_u32 s66, s66, %[lds]\n"
        "s_add_u32 s66, s66, 131072\n"
        "s_mov_b32 m0, s66\n"
        "s_movk_i32 s51, 0x1000\n"
        "s_movk_i32 s52, 0x2000\n"
        "s_getpc_b64 s[48:49]\n"
        "s_lshl_b32 s53, %[j], 7\n"
        "s_add_u32 s48, s48, s53\n"
        "s_addc_u32 s49, s49, 0\n"
        "v_mbcnt_lo_u32_b32 v164, -1, 0\n"
        "v_mbcnt_hi_u32_b32 v164, -1, v164\n"
        "v_and_b32 v172, 12, v164\n"
        "v_lshrrev_b32 v167, 4, v164\n"
        "v_lshl_add_u32 v172, v172, 1, v167\n"
        "v_and_b32 v176, 3, v164\n"
        "v_lshl_add_u32 v179, v176, 4, s58\n"
        "v_add_u32 v173, 4, v172\n"
        "v_add_u32 v174, 32, v172\n"
        "v_add_u32 v175, 36, v172\n"
        "v_lshl_add_u32 v172, v172, s57, v179\n"
        "v_lshl_add_u32 v173, v173, s57, v179\n"
        "v_lshl_add_u32 v174, v174, s57, v179\n"
        "v_lshl_add_u32 v175, v175, s57, v179\n"
        "v_xor_b32 v180, v167, v164\n"
        "v_xor_b32 v183, s56, v180\n"
        "v_lshl_add_u32 v155, v183, 4, s62\n"
        "v_and_b32 v180, 7, v180\n"
        "v_lshrrev_b32 v182, 5, v164\n"
        "v_lshl_add_u32 v180, v182, 3, v180\n"
        "v_add_u32 v180, 0x300, v180\n"
        "v_bfe_u32 v181, v164, 3, 2\n"
        "s_waitcnt lgkmcnt(0)\n"
        "global_load_dwordx4 v[184:187], v172, s[64:65]\n"
        "global_load_dwordx4 v[188:191], v173, s[64:65]\n"
        "global_load_dwordx4 v[192:195], v174, s[64:65]\n"
        "global_load_dwordx4 v[196:199], v175, s[64:65]\n"
        "s_load_dword s69, s[48:49], 0x100\n"
        "s_load_dword s69, s[48:49], 0x500\n"
        "s_load_dword s69, s[48:49], 0x900\n"
        "s_load_dword s69, s[48:49], 0xd00\n"
        "s_load_dword s69, s[48:49], 0x1100\n"
        "s_load_dword s69, s[48:49], 0x1500\n"
        "s_load_dword s69, s[48:49], 0x1900\n"
        "s_load_dword s69, s[48:49], 0x1d00\n"
        "v_mul_lo_u32 v181, v181, s36\n"
        "s_add_u32 s38, s42, s60\n"
        "s_addc_u32 s39, s43, s61\n"
        "s_add_u32 s40, s40, s60\n"
        "s_addc_u32 s41, s41, s61\n"
        "v_lshl_add_u32 v181, v180, 4, v181\n"
        "s_mov_b32 s59, s37\n"
        "global_load_lds_dwordx4 v181, s[40:41]\n"
        "v_xor_b32 v181, 64, v181\n"
        "s_add_u32 m0, s66, 768\n"
        "s_lshr_b32 s37, s34, 1\n"
        "global_load_lds_dwordx4 v181, s[40:41] offset:256\n"
        "s_mov_b32 s42, 0x3000\n"
        "s_mov_b32 s43, 0x20000\n"
        "s_barrier\n"
        "s_cmp_eq_u32 s37, 0\n"
        "s_cbranch_scc0 5f\n"
        "s_mov_b32 m0, s50\n"
        "s_nop 0\n"
        "buffer_load_dwordx4 v155, s[40:43], 0 offen offset:0 nt lds\n"
        "buffer_load_dwordx4 v155, s[40:43], 0 offen offset:2048 nt lds\n"
        "s_add_u32 m0, s50, 4096\n"
        "s_nop 0\n"
        "buffer_load_dwordx4 v155, s[40:43], s51 offen offset:0 nt lds\n"
        "buffer_load_dwordx4 v155, s[40:43], s51 offen offset:2048 nt lds\n"
        "s_add_u32 m0, s50, 8192\n"
        "s_nop 0\n"
        "buffer_load_dwordx4 v155, s[40:43], s52 offen offset:0 nt lds\n"
        "buffer_load_dwordx4 v155, s[40:43], s52 offen offset:2048 nt lds\n"
        "5:\n"
        "s_lshl_b32 s56, s35, 6\n"
        "s_add_u32 s38, s38, s56\n"
        "s_addc_u32 s39, s39, 0\n"
        "s_mov_b64 s[44:45], s[38:39]\n"
        "s_mov_b64 s[46:47], s[42:43]\n"
        "s_mov_b32 s49, 0\n"
        "s_mov_b32 s53, 49152\n"
        "s_cmp_eq_u32 s34, 2\n"
        "s_cselect_b32 s58, 0x3e8293ee, 1.0\n"
        "s_cmp_eq_u32 s34, 1\n"
        "s_cselect_b32 s58, s59, s58\n"
        "s_lshl_b32 s60, s35, 14\n"
        "s_add_u32 s60, s60, %[lds]\n"
        "s_add_u32 s60, s60, 98304\n"
        "s_lshl_b32 s61, s34, 12\n"
        "s_add_u32 s61, s61, s60\n"
        "s_mul_i32 s62, %[j], 1600\n"
        "s_add_u32 s62, s62, %[lds]\n"
        "s_add_u32 s62, s62, 147456\n"
        "v_and_b32 v165, 15, v164\n"
        "v_and_b32 v166, 48, v164\n"
        "v_lshrrev_b32 v177, 2, v165\n"
        "v_cmp_eq_u32 vcc, v167, v176\n"
        "v_and_b32 v204, 4, v165\n"
        "v_lshlrev_b32 v204, 2, v204\n"
        "v_mov_b32 v205, 0x3f80\n"
        "v_cndmask_b32 v205, 0, v205, vcc\n"
        "v_lshlrev_b32 v205, v204, v205\n"
        "v_cmp_gt_u32 vcc, 8, v165\n"
        "v_lshl_add_u32 v206, v176, 2, v167\n"
        "v_lshl_add_u32 v206, v177, 4, v206\n"
        "v_cndmask_b32 v200, 0, v205, vcc\n"
        "v_sub_u32 v201, v205, v200\n"
        "v_lshl_add_u32 v202, v206, 4, s61\n"
        "v_lshl_add_u32 v168, v164, 4, s60\n"
        "v_bfe_u32 v208, v165, 1, 3\n"
        "v_lshlrev_b32 v208, 4, v208\n"
        "v_xor_b32 v208, v208, v166\n"
        "v_lshl_add_u32 v208, v165, 7, v208\n"
        "v_add_u32 v152, s63, v208\n"
        "v_xor_b32 v153, 64, v152\n"
        "v_add_u32 v169, s66, v208\n"
        "v_xor_b32 v170, 64, v169\n"
        "v_lshl_add_u32 v154, v165, 7, v166\n"
        "v_mul_lo_u32 v158, v176, s36\n"
        "v_lshl_add_u32 v210, v177, 7, v166\n"
        "v_add3_u32 v158, v158, v210, s42\n"
        "v_mul_u32_u24 v212, 20, v166\n"
        "v_lshl_add_u32 v212, v165, 2, v212\n"
        "v_add_u32 v156, s62, v212\n"
        "v_cmp_eq_u32 s[54:55], 0, v176\n"
        "v_lshl_add_u32 v214, v164, 2, s62\n"
        "v_mov_b32 v171, s62\n"
        "v_cndmask_b32 v213, 4, v177, s[54:55]\n"
        "v_mul_u32_u24 v213, 320, v213\n"
        "v_add3_u32 v157, v213, v166, s62\n"
        "v_mov_b32 v124, 0x27802780\n"
        "v_cndmask_b32 v124, 0, v124, s[54:55]\n"
        "v_mov_b32 v125, v124\n"
        "v_mov_b64 v[126:127], v[124:125]\n"
        "v_mov_b64 v[136:137], 0\n"
        "v_mov_b64 v[138:139], 0\n"
        "v_mov_b64 v[140:141], 0\n"
        "v_mov_b64 v[142:143], 0\n"
        "v_mov_b64 v[144:145], 0\n"
        "v_mov_b64 v[146:147], 0\n"
        "v_mov_b32 v149, 0\n"
        "v_mov_b32 v151, 0\n"
        "v_mov_b32 v159, 0\n"
        "v_mov_b32 v160, -1\n"
        "ds_write_b32 v214, v149 offset:1280\n"
        "ds_write_b32 v171, v149 offset:1596\n"
        "s_waitcnt vmcnt(8)\n"
        "s_cmp_eq_u32 s37, 0\n"
        "s_cbranch_scc1 9f\n"
        "s_waitcnt vmcnt(2)\n"
        "9:\n"
        "v_mfma_f32_16x16x16_bf16 v[204:207], v[200:201], v[184:185], 0\n"
        "v_mfma_f32_16x16x16_bf16 v[208:211], v[200:201], v[188:189], 0\n"
        "v_mfma_f32_16x16x16_bf16 v[212:215], v[200:201], v[186:187], 0\n"
        "v_mfma_f32_16x16x16_bf16 v[216:219], v[200:201], v[190:191], 0\n"
        "v_mfma_f32_16x16x16_bf16 v[220:223], v[200:201], v[192:193], 0\n"
        "v_mfma_f32_16x16x16_bf16 v[224:227], v[200:201], v[196:197], 0\n"
        "v_mfma_f32_16x16x16_bf16 v[228:231], v[200:201], v[194:195], 0\n"
        "v_mfma_f32_16x16x16_bf16 v[232:235], v[200:201], v[198:199], 0\n"
        "s_nop 0\n"
        "v_mul_f32 v204, s58, v204\n"
        "v_mul_f32 v205, s58, v205\n"
        "v_mul_f32 v206, s58, v206\n"
        "v_mul_f32 v207, s58, v207\n"
        "v_mul_f32 v208, s58, v208\n"
        "v_mul_f32 v209, s58, v209\n"
        "v_mul_f32 v210, s58, v210\n"
        "v_mul_f32 v211, s58, v211\n"
        "v_cvt_pk_bf16_f32 v236, v204, v205\n"
        "v_cvt_pk_bf16_f32 v237, v206, v207\n"
        "v_cvt_pk_bf16_f32 v238, v208, v209\n"
        "v_cvt_pk_bf16_f32 v239, v210, v211\n"
        "ds_write_b128 v202, v[236:239] offset:0\n"
        "v_mul_f32 v212, s58, v212\n"
        "v_mul_f32 v213, s58, v213\n"
        "v_mul_f32 v214, s58, v214\n"
        "v_mul_f32 v215, s58, v215\n"
        "v_mul_f32 v216, s58, v216\n"
        "v_mul_f32 v217, s58, v217\n"
        "v_mul_f32 v218, s58, v218\n"
        "v_mul_f32 v219, s58, v219\n"
        "v_cvt_pk_bf16_f32 v240, v212, v213\n"
        "v_cvt_pk_bf16_f32 v241, v214, v215\n"
        "v_cvt_pk_bf16_f32 v242, v216, v217\n"
        "v_cvt_pk_bf16_f32 v243, v218, v219\n"
        "ds_write_b128 v202, v[240:243] offset:2048\n"
        "v_mul_f32 v220, s58, v220\n"
        "v_mul_f32 v221, s58, v221\n"
        "v_mul_f32 v222, s58, v222\n"
        "v_mul_f32 v223, s58, v223\n"
        "v_mul_f32 v224, s58, v224\n"
        "v_mul_f32 v225, s58, v225\n"
        "v_mul_f32 v226, s58, v226\n"
        "v_mul_f32 v227, s58, v227\n"
        "v_cvt_pk_bf16_f32 v244, v220, v221\n"
        "v_cvt_pk_bf16_f32 v245, v222, v223\n"
        "v_cvt_pk_bf16_f32 v246, v224, v225\n"
        "v_cvt_pk_bf16_f32 v247, v226, v227\n"
        "ds_write_b128 v202, v[244:247] offset:1024\n"
        "v_mul_f32 v228, s58, v228\n"
        "v_mul_f32 v229, s58, v229\n"
        "v_mul_f32 v230, s58, v230\n"
        "v_mul_f32 v231, s58, v231\n"
        "v_mul_f32 v232, s58, v232\n"
        "v_mul_f32 v233, s58, v233\n"
        "v_mul_f32 v234, s58, v234\n"
        "v_mul_f32 v235, s58, v235\n"
        "v_cvt_pk_bf16_f32 v64, v228, v229\n"
        "v_cvt_pk_bf16_f32 v65, v230, v231\n"
        "v_cvt_pk_bf16_f32 v66, v232, v233\n"
        "v_cvt_pk_bf16_f32 v67, v234, v235\n"
        "ds_write_b128 v202, v[64:67] offset:3072\n"
        "s_waitcnt lgkmcnt(0)\n"
        "s_barrier\n"
        "ds_read_b128 v[32:35], v168 offset:8192\n"
        "ds_read_b128 v[40:43], v168 offset:10240\n"
        "ds_read_b128 v[36:39], v168 offset:9216\n"
        "ds_read_b128 v[44:47], v168 offset:11264\n"
        "ds_read_b128 v[0:3], v168 offset:0\n"
        "ds_read_b128 v[8:11], v168 offset:2048\n"
        "ds_read_b128 v[4:7], v168 offset:1024\n"
        "ds_read_b128 v[12:15], v168 offset:3072\n"
        "ds_read_b128 v[16:19], v168 offset:4096\n"
        "ds_read_b128 v[24:27], v168 offset:6144\n"
        "ds_read_b128 v[20:23], v168 offset:5120\n"
        "ds_read_b128 v[28:31], v168 offset:7168\n"
        "ds_read_b128 v[48:51], v168 offset:12288\n"
        "ds_read_b128 v[56:59], v168 offset:14336\n"
        "ds_read_b128 v[52:55], v168 offset:13312\n"
        "ds_read_b128 v[60:63], v168 offset:15360\n"
        "s_waitcnt vmcnt(6)\n"
        "s_cmp_eq_u32 s37, 0\n"
        "s_cbranch_scc1 9f\n"
        "s_waitcnt vmcnt(0)\n"
        "9:\n"
        "ds_read_b128 v[204:207], v169\n"
        "ds_read_b128 v[208:211], v170\n"
        "s_cbranch_scc0 13f\n"
        "6:\n"
        "s_waitcnt lgkmcnt(1)\n"
        "v_mfma_f32_16x16x32_bf16 v[212:215], v[32:35], v[204:207], 0\n"
        "v_mfma_f32_16x16x32_bf16 v[216:219], v[40:43], v[204:207], 0\n"
        "s_waitcnt lgkmcnt(0)\n"
        "v_mfma_f32_16x16x32_bf16 v[212:215], v[36:39], v[208:211], v[212:215]\n"
        "v_mfma_f32_16x16x32_bf16 v[216:219], v[44:47], v[208:211], v[216:219]\n"
        "v_mfma_f32_16x16x32_bf16 v[220:223], v[0:3], v[204:207], 0\n"
        "v_mfma_f32_16x16x32_bf16 v[224:227], v[8:11], v[204:207], 0\n"
        "v_mfma_f32_16x16x32_bf16 v[220:223], v[4:7], v[208:211], v[220:223]\n"
        "v_mfma_f32_16x16x32_bf16 v[224:227], v[12:15], v[208:211], v[224:227]\n"
        "v_mfma_f32_16x16x32_bf16 v[228:231], v[204:207], v[16:19], 0\n"
        "v_mfma_f32_16x16x32_bf16 v[232:235], v[204:207], v[24:27], 0\n"
        "v_mfma_f32_16x16x32_bf16 v[228:231], v[208:211], v[20:23], v[228:231]\n"
        "v_cvt_pk_bf16_f32 v116, v212, v213\n"
        "v_cvt_pk_bf16_f32 v117, v214, v215\n"
        "v_mfma_f32_16x16x32_bf16 v[232:235], v[208:211], v[28:31], v[232:235]\n"
        "v_cvt_pk_bf16_f32 v118, v216, v217\n"
        "v_cvt_pk_bf16_f32 v119, v218, v219\n"
        "v_mfma_f32_16x16x32_bf16 v[128:131], v[48:51], v[204:207], 0\n"
        "v_cvt_pk_bf16_f32 v112, v220, v221\n"
        "v_cvt_pk_bf16_f32 v113, v222, v223\n"
        "v_mfma_f32_16x16x32_bf16 v[132:135], v[56:59], v[204:207], 0\n"
        "v_cvt_pk_bf16_f32 v114, v224, v225\n"
        "v_cvt_pk_bf16_f32 v115, v226, v227\n"
        "v_mfma_f32_16x16x32_bf16 v[128:131], v[52:55], v[208:211], v[128:131]\n"
        "v_cvt_pk_bf16_f32 v120, v228, v229\n"
        "v_cvt_pk_bf16_f32 v121, v230, v231\n"
        "v_mfma_f32_16x16x32_bf16 v[132:135], v[60:63], v[208:211], v[132:135]\n"
        "v_cvt_pk_bf16_f32 v122, v232, v233\n"
        "v_cvt_pk_bf16_f32 v123, v234, v235\n"
        "s_cmp_eq_u32 s37, 0\n"
        "s_cbranch_scc0 8f\n"
        "s_add_u32 s40, s40, s36\n"
        "s_addc_u32 s41, s41, 0\n"
        "s_add_u32 s50, s50, 49152\n"
        "s_waitcnt vmcnt(0) lgkmcnt(0)\n"
        "s_barrier\n"
        "12:\n"
        "ds_read_b128 v[248:251], v152 offset:0\n"
        "ds_read_b128 v[252:255], v153 offset:0\n"
        "1:\n"
        "2:\n"
        "s_add_u32 s40, s40, s36\n"
        "s_addc_u32 s41, s41, 0\n"
        "s_cmp_eq_u32 s49, 2\n"
        "s_cselect_b32 s42, 0, s42\n"
        "s_cselect_b32 s48, -98304, s53\n"
        "s_cmp_eq_u32 s49, 1\n"
        "s_cselect_b32 s58, -98304, s53\n"
        "s_add_u32 s50, s50, s58\n"
        "s_add_u32 s60, s50, 0x1000\n"
        "s_add_u32 s61, s50, 0x2000\n"
        "s_waitcnt lgkmcnt(1)\n"
        "v_mfma_f32_16x16x32_bf16 v[164:167], v[248:251], v[16:19], 0\n"
        "ds_read_b128 v[168:171], v152 offset:4096\n"
        "ds_read_b128 v[172:175], v153 offset:4096\n"
        "ds_read_b128 v[176:179], v152 offset:2048\n"
        "v_mfma_f32_16x16x32_bf16 v[180:183], v[248:251], v[24:27], 0\n"
        "s_mov_b32 m0, s50\n"
        "s_nop 0\n"
        "buffer_load_dwordx4 v155, s[40:43], 0 offen offset:0 nt lds\n"
        "ds_read_b128 v[184:187], v153 offset:2048\n"
        "s_waitcnt lgkmcnt(4)\n"
        "v_mfma_f32_16x16x32_bf16 v[164:167], v[252:255], v[20:23], v[164:167]\n"
        "v_mfma_f32_16x16x32_bf16 v[180:183], v[252:255], v[28:31], v[180:183]\n"
        "v_mfma_f32_16x16x32_bf16 v[188:191], v[0:3], v[248:251], 0\n"
        "v_mfma_f32_16x16x32_bf16 v[192:195], v[8:11], v[248:251], 0\n"
        "v_mfma_f32_16x16x32_bf16 v[188:191], v[4:7], v[252:255], v[188:191]\n"
        "v_mfma_f32_16x16x32_bf16 v[192:195], v[12:15], v[252:255], v[192:195]\n"
        "v_mfma_f32_16x16x32_bf16 v[196:199], v[32:35], v[248:251], 0\n"
        "v_mfma_f32_16x16x32_bf16 v[200:203], v[40:43], v[248:251], 0\n"
        "s_waitcnt lgkmcnt(1)\n"
        "v_mfma_f32_16x16x32_bf16 v[204:207], v[32:35], v[176:179], 0\n"
        "v_mfma_f32_16x16x32_bf16 v[208:211], v[40:43], v[176:179], 0\n"
        "s_waitcnt lgkmcnt(0)\n"
        "v_mfma_f32_16x16x32_bf16 v[204:207], v[36:39], v[184:187], v[204:207]\n"
        "v_mfma_f32_16x16x32_bf16 v[208:211], v[44:47], v[184:187], v[208:211]\n"
        "v_mfma_f32_16x16x32_bf16 v[196:199], v[36:39], v[252:255], v[196:199]\n"
        "v_cvt_pk_bf16_f32 v64, v188, v189\n"
        "v_cvt_pk_bf16_f32 v65, v190, v191\n"
        "v_mfma_f32_16x16x32_bf16 v[200:203], v[44:47], v[252:255], v[200:203]\n"
        "v_cvt_pk_bf16_f32 v66, v192, v193\n"
        "v_cvt_pk_bf16_f32 v67, v194, v195\n"
        "v_mfma_f32_16x16x32_bf16 v[212:215], v[0:3], v[168:171], 0\n"
        "v_cvt_pk_bf16_f32 v216, v204, v205\n"
        "v_cvt_pk_bf16_f32 v217, v206, v207\n"
        "v_mfma_f32_16x16x32_bf16 v[220:223], v[8:11], v[168:171], 0\n"
        "v_cvt_pk_bf16_f32 v218, v208, v209\n"
        "v_cvt_pk_bf16_f32 v219, v210, v211\n"
        "v_mfma_f32_16x16x32_bf16 v[212:215], v[4:7], v[172:175], v[212:215]\n"
        "v_cvt_pk_bf16_f32 v224, v196, v197\n"
        "v_cvt_pk_bf16_f32 v225, v198, v199\n"
        "v_mfma_f32_16x16x32_bf16 v[220:223], v[12:15], v[172:175], v[220:223]\n"
        "v_cvt_pk_bf16_f32 v226, v200, v201\n"
        "v_cvt_pk_bf16_f32 v227, v202, v203\n"
        "v_mfma_f32_16x16x32_bf16 v[228:231], v[0:3], v[176:179], 0\n"
        "ds_read_b128 v[232:235], v152 offset:6144\n"
        "ds_read_b128 v[236:239], v153 offset:6144\n"
        "v_cvt_pk_bf16_f32 v72, v212, v213\n"
        "v_mfma_f32_16x16x32_bf16 v[240:243], v[64:67], v[224:227], 0\n"
        "v_cvt_pk_bf16_f32 v73, v214, v215\n"
        "v_cvt_pk_bf16_f32 v74, v220, v221\n"
        "v_mfma_f32_16x16x32_bf16 v[244:247], v[8:11], v[176:179], 0\n"
        "v_cvt_pk_bf16_f32 v75, v222, v223\n"
        "v_mfma_f32_16x16x32_bf16 v[228:231], v[4:7], v[184:187], v[228:231]\n"
        "v_mfma_f32_16x16x32_bf16 v[244:247], v[12:15], v[184:187], v[244:247]\n"
        "v_mfma_f32_16x16x32_bf16 v[188:191], v[72:75], v[224:227], 0\n"
        "v_mfma_f32_16x16x32_bf16 v[192:195], v[116:119], v[72:75], 0\n"
        "v_exp_f32 v240, v240\n"
        "v_mfma_f32_16x16x32_bf16 v[204:207], v[48:51], v[248:251], 0\n"
        "v_exp_f32 v241, v241\n"
        "v_mfma_f32_16x16x32_bf16 v[208:211], v[56:59], v[248:251], 0\n"
        "v_exp_f32 v242, v242\n"
        "s_waitcnt lgkmcnt(1)\n"
        "v_mfma_f32_16x16x32_bf16 v[196:199], v[0:3], v[232:235], 0\n"
        "v_exp_f32 v243, v243\n"
        "v_mfma_f32_16x16x32_bf16 v[200:203], v[8:11], v[232:235], 0\n"
        "v_exp_f32 v192, v192\n"
        "s_waitcnt lgkmcnt(0)\n"
        "v_mfma_f32_16x16x32_bf16 v[196:199], v[4:7], v[236:239], v[196:199]\n"
        "v_cvt_pk_bf16_f32 v68, v228, v229\n"
        "v_cvt_pk_bf16_f32 v69, v230, v231\n"
        "v_mfma_f32_16x16x32_bf16 v[200:203], v[12:15], v[236:239], v[200:203]\n"
        "v_cvt_pk_bf16_f32 v70, v244, v245\n"
        "v_cvt_pk_bf16_f32 v71, v246, v247\n"
        "v_mfma_f32_16x16x32_bf16 v[212:215], v[232:235], v[16:19], 0\n"
        "v_exp_f32 v188, v188\n"
        "v_mfma_f32_16x16x32_bf16 v[220:223], v[68:71], v[224:227], 0\n"
        "v_cvt_pk_bf16_f32 v76, v196, v197\n"
        "v_cvt_pk_bf16_f32 v77, v198, v199\n"
        "v_mfma_f32_16x16x32_bf16 v[248:251], v[116:119], v[68:71], 0\n"
        "v_cvt_pk_bf16_f32 v78, v200, v201\n"
        "v_cvt_pk_bf16_f32 v79, v202, v203\n"
        "v_mfma_f32_16x16x32_bf16 v[228:231], v[232:235], v[24:27], 0\n"
        "v_exp_f32 v189, v189\n"
        "v_mfma_f32_16x16x32_bf16 v[244:247], v[116:119], v[76:79], 0\n"
        "v_exp_f32 v220, v220\n"
        "v_mfma_f32_16x16x32_bf16 v[212:215], v[236:239], v[20:23], v[212:215]\n"
        "v_exp_f32 v221, v221\n"
        "v_mfma_f32_16x16x32_bf16 v[228:231], v[236:239], v[28:31], v[228:231]\n"
        "v_exp_f32 v222, v222\n"
        "v_mfma_f32_16x16x32_bf16 v[204:207], v[52:55], v[252:255], v[204:207]\n"
        "v_exp_f32 v223, v223\n"
        "v_mfma_f32_16x16x32_bf16 v[208:211], v[60:63], v[252:255], v[208:211]\n"
        "v_exp_f32 v244, v244\n"
        "v_mfma_f32_16x16x32_bf16 v[196:199], v[76:79], v[224:227], 0\n"
        "v_cvt_pk_bf16_f32 v245, v192, v244\n"
        "v_exp_f32 v248, v248\n"
        "v_mfma_f32_16x16x32_bf16 v[200:203], v[116:119], v[64:67], 0\n"
        "v_cvt_pk_bf16_f32 v232, v240, v220\n"
        "v_cvt_pk_bf16_f32 v233, v241, v221\n"
        "v_mfma_f32_16x16x32_bf16 v[236:239], v[168:171], v[16:19], 0\n"
        "v_cvt_pk_bf16_f32 v234, v242, v222\n"
        "v_cvt_pk_bf16_f32 v235, v243, v223\n"
        "v_mfma_f32_16x16x32_bf16 v[252:255], v[168:171], v[24:27], 0\n"
        "v_exp_f32 v190, v190\n"
        "v_mfma_f32_16x16x32_bf16 v[192:195], v[124:127], v[232:235], 0\n"
        "v_exp_f32 v200, v200\n"
        "v_mfma_f32_16x16x32_bf16 v[240:243], v[48:51], v[176:179], 0\n"
        "v_cvt_pk_bf16_f32 v249, v200, v248\n"
        "v_exp_f32 v191, v191\n"
        "ds_write_b32 v156, v249 offset:0\n"
        "v_mfma_f32_16x16x32_bf16 v[236:239], v[172:175], v[20:23], v[236:239]\n"
        "ds_write_b32 v156, v245 offset:64\n"
        "v_exp_f32 v196, v196\n"
        "v_mfma_f32_16x16x32_bf16 v[220:223], v[56:59], v[176:179], 0\n"
        "v_exp_f32 v197, v197\n"
        "v_mfma_f32_16x16x32_bf16 v[252:255], v[172:175], v[28:31], v[252:255]\n"
        "v_exp_f32 v198, v198\n"
        "v_mfma_f32_16x16x32_bf16 v[240:243], v[52:55], v[184:187], v[240:243]\n"
        "v_exp_f32 v199, v199\n"
        "v_mfma_f32_16x16x32_bf16 v[220:223], v[60:63], v[184:187], v[220:223]\n"
        "v_cvt_pk_bf16_f32 v96, v236, v212\n"
        "v_cvt_pk_bf16_f32 v97, v237, v213\n"
        "v_mfma_f32_16x16x32_bf16 v[168:171], v[176:179], v[16:19], 0\n"
        "v_cvt_pk_bf16_f32 v98, v238, v214\n"
        "v_cvt_pk_bf16_f32 v99, v239, v215\n"
        "v_mfma_f32_16x16x32_bf16 v[200:203], v[176:179], v[24:27], 0\n"
        "buffer_load_dwordx4 v155, s[40:43], 0 offen offset:2048 nt lds\n"
        "v_cvt_pk_bf16_f32 v100, v252, v228\n"
        "v_cvt_pk_bf16_f32 v101, v253, v229\n"
        "v_mfma_f32_16x16x32_bf16 v[168:171], v[184:187], v[20:23], v[168:171]\n"
        "v_cvt_pk_bf16_f32 v102, v254, v230\n"
        "v_cvt_pk_bf16_f32 v103, v255, v231\n"
        "v_mfma_f32_16x16x32_bf16 v[200:203], v[184:187], v[28:31], v[200:203]\n"
        "ds_read_b128 v[248:251], v152 offset:8192\n"
        "v_cvt_pk_bf16_f32 v244, v188, v196\n"
        "v_cvt_pk_bf16_f32 v245, v189, v197\n"
        "v_mfma_f32_16x16x32_bf16 v[172:175], v[116:119], v[112:115], 0\n"
        "ds_read_b128 v[236:239], v153 offset:8192\n"
        "v_cvt_pk_bf16_f32 v88, v164, v168\n"
        "v_cvt_pk_bf16_f32 v89, v165, v169\n"
        "v_mfma_f32_16x16x32_bf16 v[212:215], v[68:71], v[216:219], 0\n"
        "v_cvt_pk_bf16_f32 v90, v166, v170\n"
        "v_cvt_pk_bf16_f32 v91, v167, v171\n"
        "v_mfma_f32_16x16x32_bf16 v[176:179], v[72:75], v[216:219], 0\n"
        "v_cvt_pk_bf16_f32 v92, v180, v200\n"
        "v_cvt_pk_bf16_f32 v93, v181, v201\n"
        "v_mfma_f32_16x16x32_bf16 v[252:255], v[76:79], v[216:219], 0\n"
        "v_cvt_pk_bf16_f32 v94, v182, v202\n"
        "v_cvt_pk_bf16_f32 v95, v183, v203\n"
        "v_mfma_f32_16x16x32_bf16 v[228:231], v[88:91], v[232:235], 0\n"
        "ds_read_b128 v[184:187], v152 offset:10240\n"
        "v_exp_f32 v212, v212\n"
        "ds_read_b128 v[164:167], v153 offset:10240\n"
        "v_mfma_f32_16x16x32_bf16 v[168:171], v[92:95], v[232:235], 0\n"
        "v_exp_f32 v213, v213\n"
        "v_mfma_f32_16x16x32_bf16 v[180:183], v[112:115], v[216:219], 0\n"
        "v_exp_f32 v214, v214\n"
        "v_mfma_f32_16x16x32_bf16 v[200:203], v[112:115], v[224:227], 0\n"
        "v_exp_f32 v215, v215\n"
        "v_cvt_pk_bf16_f32 v246, v190, v198\n"
        "v_exp_f32 v172, v172\n"
        "v_cvt_pk_bf16_f32 v247, v191, v199\n"
        "v_cvt_pk_bf16_f32 v173, v172, 0\n"
        "v_cndmask_b32 v173, 0, v173, s[54:55]\n"
        "s_waitcnt lgkmcnt(3)\n"
        "v_mfma_f32_16x16x32_bf16 v[232:235], v[0:3], v[248:251], 0\n"
        "v_exp_f32 v180, v180\n"
        "v_mfma_f32_16x16x32_bf16 v[188:191], v[8:11], v[248:251], 0\n"
        "v_exp_f32 v200, v200\n"
        "s_waitcnt lgkmcnt(2)\n"
        "v_mfma_f32_16x16x32_bf16 v[232:235], v[4:7], v[236:239], v[232:235]\n"
        "v_cvt_pk_bf16_f32 v148, v200, 0\n"
        "v_cvt_pk_bf16_f32 v150, v180, 0\n"
        "v_mfma_f32_16x16x32_bf16 v[188:191], v[12:15], v[236:239], v[188:191]\n"
        "v_exp_f32 v176, v176\n"
        "s_waitcnt lgkmcnt(1)\n"
        "v_mfma_f32_16x16x32_bf16 v[196:199], v[0:3], v[184:187], 0\n"
        "v_exp_f32 v177, v177\n"
        "v_mfma_f32_16x16x32_bf16 v[200:203], v[8:11], v[184:187], 0\n"
        "v_exp_f32 v178, v178\n"
        "s_waitcnt lgkmcnt(0)\n"
        "v_mfma_f32_16x16x32_bf16 v[196:199], v[4:7], v[164:167], v[196:199]\n"
        "v_cvt_pk_bf16_f32 v80, v232, v233\n"
        "v_cvt_pk_bf16_f32 v81, v234, v235\n"
        "v_mfma_f32_16x16x32_bf16 v[200:203], v[12:15], v[164:167], v[200:203]\n"
        "s_setprio 1\n"
        "v_cvt_pk_bf16_f32 v82, v188, v189\n"
        "v_cvt_pk_bf16_f32 v83, v190, v191\n"
        "v_mfma_f32_16x16x32_bf16 v[180:183], v[184:187], v[16:19], 0\n"
        "v_exp_f32 v179, v179\n"
        "v_mfma_f32_16x16x32_bf16 v[232:235], v[80:83], v[216:219], 0\n"
        "v_cvt_pk_bf16_f32 v84, v196, v197\n"
        "v_cvt_pk_bf16_f32 v85, v198, v199\n"
        "v_mfma_f32_16x16x32_bf16 v[180:183], v[164:167], v[20:23], v[180:183]\n"
        "v_cvt_pk_bf16_f32 v86, v200, v201\n"
        "v_cvt_pk_bf16_f32 v87, v202, v203\n"
        "v_mfma_f32_16x16x32_bf16 v[188:191], v[116:119], v[80:83], 0\n"
        "v_exp_f32 v252, v252\n"
        "v_mfma_f32_16x16x32_bf16 v[196:199], v[84:87], v[224:227], 0\n"
        "v_exp_f32 v253, v253\n"
        "v_mfma_f32_16x16x32_bf16 v[228:231], v[96:99], v[244:247], v[228:231]\n"
        "v_exp_f32 v254, v254\n"
        "v_mfma_f32_16x16x32_bf16 v[168:171], v[100:103], v[244:247], v[168:171]\n"
        "v_exp_f32 v255, v255\n"
        "v_mfma_f32_16x16x32_bf16 v[192:195], v[124:127], v[244:247], v[192:195]\n"
        "v_exp_f32 v188, v188\n"
        "v_mfma_f32_16x16x32_bf16 v[200:203], v[248:251], v[16:19], 0\n"
        "v_exp_f32 v196, v196\n"
        "v_mfma_f32_16x16x32_bf16 v[244:247], v[248:251], v[24:27], 0\n"
        "v_exp_f32 v197, v197\n"
        "v_mfma_f32_16x16x32_bf16 v[200:203], v[236:239], v[20:23], v[200:203]\n"
        "v_exp_f32 v198, v198\n"
        "v_mfma_f32_16x16x32_bf16 v[244:247], v[236:239], v[28:31], v[244:247]\n"
        "v_exp_f32 v199, v199\n"
        "v_mfma_f32_16x16x32_bf16 v[248:251], v[184:187], v[24:27], 0\n"
        "v_exp_f32 v232, v232\n"
        "v_mfma_f32_16x16x32_bf16 v[248:251], v[164:167], v[28:31], v[248:251]\n"
        "ds_read_b128 v[236:239], v152 offset:4096\n"
        "v_exp_f32 v233, v233\n"
        "ds_read_b128 v[184:187], v153 offset:4096\n"
        "v_mfma_f32_16x16x32_bf16 v[164:167], v[116:119], v[84:87], 0\n"
        "v_cvt_pk_bf16_f32 v104, v200, v180\n"
        "v_cvt_pk_bf16_f32 v105, v201, v181\n"
        "v_cvt_pk_bf16_f32 v106, v202, v182\n"
        "v_cvt_pk_bf16_f32 v107, v203, v183\n"
        "v_cvt_pk_bf16_f32 v108, v244, v248\n"
        "v_cvt_pk_bf16_f32 v109, v245, v249\n"
        "v_mfma_f32_16x16x32_bf16 v[200:203], v[80:83], v[224:227], 0\n"
        "v_cvt_pk_bf16_f32 v110, v246, v250\n"
        "v_exp_f32 v164, v164\n"
        "v_mfma_f32_16x16x32_bf16 v[180:183], v[84:87], v[216:219], 0\n"
        "v_cvt_pk_bf16_f32 v165, v188, v164\n"
        "v_cvt_pk_bf16_f32 v111, v247, v251\n"
        "v_exp_f32 v234, v234\n"
        "ds_write_b32 v156, v165 offset:128\n"
        "v_mfma_f32_16x16x32_bf16 v[224:227], v[64:67], v[216:219], 0\n"
        "ds_write_b32 v156, v173 offset:192\n"
        "v_exp_f32 v200, v200\n"
        "v_exp_f32 v201, v201\n"
        "v_exp_f32 v202, v202\n"
        "v_exp_f32 v203, v203\n"
        "v_cvt_pk_bf16_f32 v188, v200, v196\n"
        "s_waitcnt lgkmcnt(3)\n"
        "v_mfma_f32_16x16x32_bf16 v[244:247], v[32:35], v[236:239], 0\n"
        "v_cvt_pk_bf16_f32 v189, v201, v197\n"
        "v_exp_f32 v224, v224\n"
        "v_mfma_f32_16x16x32_bf16 v[248:251], v[40:43], v[236:239], 0\n"
        "v_exp_f32 v225, v225\n"
        "s_waitcnt lgkmcnt(2)\n"
        "v_mfma_f32_16x16x32_bf16 v[244:247], v[36:39], v[184:187], v[244:247]\n"
        "v_exp_f32 v226, v226\n"
        "v_mfma_f32_16x16x32_bf16 v[248:251], v[44:47], v[184:187], v[248:251]\n"
        "s_mov_b32 m0, s60\n"
        "s_nop 0\n"
        "buffer_load_dwordx4 v155, s[40:43], s51 offen offset:0 nt lds\n"
        "v_exp_f32 v227, v227\n"
        "v_mfma_f32_16x16x32_bf16 v[164:167], v[48:51], v[236:239], 0\n"
        "v_cvt_pk_bf16_f32 v190, v202, v198\n"
        "v_cvt_pk_bf16_f32 v191, v203, v199\n"
        "v_mfma_f32_16x16x32_bf16 v[216:219], v[56:59], v[236:239], 0\n"
        "v_exp_f32 v235, v235\n"
        "v_mfma_f32_16x16x32_bf16 v[228:231], v[104:107], v[188:191], v[228:231]\n"
        "v_cvt_pk_bf16_f32 v172, v244, v245\n"
        "v_cvt_pk_bf16_f32 v173, v246, v247\n"
        "v_mfma_f32_16x16x32_bf16 v[168:171], v[108:111], v[188:191], v[168:171]\n"
        "v_cvt_pk_bf16_f32 v174, v248, v249\n"
        "v_cvt_pk_bf16_f32 v175, v250, v251\n"
        "v_mfma_f32_16x16x32_bf16 v[192:195], v[124:127], v[188:191], v[192:195]\n"
        "v_cvt_pk_bf16_f32 v200, v224, v212\n"
        "v_cvt_pk_bf16_f32 v201, v225, v213\n"
        "v_mfma_f32_16x16x16_bf16 v[228:231], v[120:121], v[148:149], v[228:231]\n"
        "v_cvt_pk_bf16_f32 v202, v226, v214\n"
        "v_cvt_pk_bf16_f32 v203, v227, v215\n"
        "v_cvt_pk_bf16_f32 v196, v176, v252\n"
        "v_mfma_f32_16x16x16_bf16 v[192:195], v[124:125], v[148:149], v[192:195]\n"
        "v_cvt_pk_bf16_f32 v197, v177, v253\n"
        "v_cvt_pk_bf16_f32 v198, v178, v254\n"
        "v_cvt_pk_bf16_f32 v199, v179, v255\n"
        "v_mfma_f32_16x16x16_bf16 v[168:171], v[122:123], v[148:149], v[168:171]\n"
        "v_exp_f32 v180, v180\n"
        "v_exp_f32 v181, v181\n"
        "v_mfma_f32_16x16x32_bf16 v[236:239], v[64:67], v[172:175], 0\n"
        "v_cvt_pk_bf16_f32 v244, v232, v180\n"
        "v_cvt_pk_bf16_f32 v245, v233, v181\n"
        "v_mfma_f32_16x16x32_bf16 v[248:251], v[68:71], v[172:175], 0\n"
        "v_rcp_f32 v161, v192\n"
        "v_mfma_f32_16x16x32_bf16 v[188:191], v[72:75], v[172:175], 0\n"
        "v_fmac_f32 v204, v228, v161\n"
        "v_fmac_f32 v205, v229, v161\n"
        "v_mfma_f32_16x16x32_bf16 v[224:227], v[80:83], v[172:175], 0\n"
        "v_fmac_f32 v206, v230, v161\n"
        "v_fmac_f32 v207, v231, v161\n"
        "v_mfma_f32_16x16x32_bf16 v[212:215], v[88:91], v[200:203], 0\n"
        "v_fmac_f32 v208, v168, v161\n"
        "v_fmac_f32 v209, v169, v161\n"
        "v_mfma_f32_16x16x32_bf16 v[176:179], v[92:95], v[200:203], 0\n"
        "v_fmac_f32 v210, v170, v161\n"
        "v_fmac_f32 v211, v171, v161\n"
        "v_mfma_f32_16x16x32_bf16 v[252:255], v[124:127], v[200:203], 0\n"
        "v_cvt_pk_bf16_f32 v192, v204, v205\n"
        "v_cvt_pk_bf16_f32 v193, v206, v207\n"
        "v_mfma_f32_16x16x32_bf16 v[228:231], v[112:115], v[172:175], 0\n"
        "v_cvt_pk_bf16_f32 v194, v208, v209\n"
        "v_cvt_pk_bf16_f32 v195, v210, v211\n"
        "v_mfma_f32_16x16x32_bf16 v[212:215], v[96:99], v[196:199], v[212:215]\n"
        "v_pk_max_i16 v192, v192, 0\n"
        "v_pk_max_i16 v193, v193, 0\n"
        "v_mfma_f32_16x16x32_bf16 v[176:179], v[100:103], v[196:199], v[176:179]\n"
        "v_pk_max_i16 v194, v194, 0\n"
        "v_pk_max_i16 v195, v195, 0\n"
        "v_mfma_f32_16x16x32_bf16 v[252:255], v[124:127], v[196:199], v[252:255]\n"
        "buffer_store_dwordx4 v[192:195], v154, s[44:47], 0 offen offset:0 sc1\n"
        "v_exp_f32 v224, v224\n"
        "s_nop 0\n"
        "v_mfma_f32_16x16x32_bf16 v[168:171], v[76:79], v[172:175], 0\n"
        "ds_read_b128 v[200:203], v152 offset:6144\n"
        "v_exp_f32 v225, v225\n"
        "ds_read_b128 v[204:207], v153 offset:6144\n"
        "v_mfma_f32_16x16x32_bf16 v[164:167], v[52:55], v[184:187], v[164:167]\n"
        "v_exp_f32 v226, v226\n"
        "v_mfma_f32_16x16x32_bf16 v[216:219], v[60:63], v[184:187], v[216:219]\n"
        "v_exp_f32 v227, v227\n"
        "v_mfma_f32_16x16x32_bf16 v[208:211], v[84:87], v[172:175], 0\n"
        "v_exp_f32 v236, v236\n"
        "v_exp_f32 v237, v237\n"
        "v_exp_f32 v238, v238\n"
        "v_exp_f32 v239, v239\n"
        "v_exp_f32 v248, v248\n"
        "v_exp_f32 v249, v249\n"
        "v_exp_f32 v250, v250\n"
        "v_exp_f32 v251, v251\n"
        "v_exp_f32 v208, v208\n"
        "v_exp_f32 v209, v209\n"
        "s_waitcnt lgkmcnt(1)\n"
        "v_mfma_f32_16x16x32_bf16 v[196:199], v[32:35], v[200:203], 0\n"
        "v_exp_f32 v210, v210\n"
        "v_mfma_f32_16x16x32_bf16 v[192:195], v[40:43], v[200:203], 0\n"
        "v_exp_f32 v211, v211\n"
        "s_waitcnt lgkmcnt(0)\n"
        "v_mfma_f32_16x16x32_bf16 v[196:199], v[36:39], v[204:207], v[196:199]\n"
        "v_cvt_pk_bf16_f32 v184, v236, v248\n"
        "v_cvt_pk_bf16_f32 v185, v237, v249\n"
        "v_mfma_f32_16x16x32_bf16 v[192:195], v[44:47], v[204:207], v[192:195]\n"
        "v_cvt_pk_bf16_f32 v186, v238, v250\n"
        "v_cvt_pk_bf16_f32 v187, v239, v251\n"
        "v_exp_f32 v182, v182\n"
        "v_exp_f32 v183, v183\n"
        "v_mfma_f32_16x16x32_bf16 v[172:175], v[88:91], v[184:187], 0\n"
        "v_cvt_pk_bf16_f32 v246, v234, v182\n"
        "v_cvt_pk_bf16_f32 v247, v235, v183\n"
        "v_mfma_f32_16x16x32_bf16 v[236:239], v[92:95], v[184:187], 0\n"
        "v_exp_f32 v188, v188\n"
        "v_mfma_f32_16x16x32_bf16 v[212:215], v[104:107], v[244:247], v[212:215]\n"
        "v_cvt_pk_bf16_f32 v248, v196, v197\n"
        "v_cvt_pk_bf16_f32 v249, v198, v199\n"
        "v_mfma_f32_16x16x32_bf16 v[176:179], v[108:111], v[244:247], v[176:179]\n"
        "v_cvt_pk_bf16_f32 v250, v192, v193\n"
        "v_cvt_pk_bf16_f32 v251, v194, v195\n"
        "v_mfma_f32_16x16x32_bf16 v[252:255], v[124:127], v[244:247], v[252:255]\n"
        "v_exp_f32 v189, v189\n"
        "v_mfma_f32_16x16x16_bf16 v[212:215], v[120:121], v[150:151], v[212:215]\n"
        "v_exp_f32 v190, v190\n"
        "v_exp_f32 v191, v191\n"
        "v_mfma_f32_16x16x16_bf16 v[176:179], v[122:123], v[150:151], v[176:179]\n"
        "v_exp_f32 v168, v168\n"
        "v_exp_f32 v169, v169\n"
        "v_mfma_f32_16x16x16_bf16 v[252:255], v[124:125], v[150:151], v[252:255]\n"
        "v_cvt_pk_bf16_f32 v232, v188, v168\n"
        "v_cvt_pk_bf16_f32 v233, v189, v169\n"
        "v_exp_f32 v170, v170\n"
        "v_mfma_f32_16x16x32_bf16 v[180:183], v[68:71], v[248:251], 0\n"
        "v_cvt_pk_bf16_f32 v234, v190, v170\n"
        "v_exp_f32 v171, v171\n"
        "v_mfma_f32_16x16x32_bf16 v[196:199], v[80:83], v[248:251], 0\n"
        "v_cvt_pk_bf16_f32 v235, v191, v171\n"
        "v_rcp_f32 v162, v252\n"
        "v_mfma_f32_16x16x32_bf16 v[192:195], v[124:127], v[184:187], 0\n"
        "v_fmac_f32 v240, v212, v162\n"
        "v_fmac_f32 v241, v213, v162\n"
        "v_mfma_f32_16x16x32_bf16 v[172:175], v[96:99], v[232:235], v[172:175]\n"
        "buffer_load_dwordx4 v155, s[40:43], s51 offen offset:2048 nt lds\n"
        "v_fmac_f32 v242, v214, v162\n"
        "v_fmac_f32 v243, v215, v162\n"
        "v_mfma_f32_16x16x32_bf16 v[236:239], v[100:103], v[232:235], v[236:239]\n"
        "v_fmac_f32 v220, v176, v162\n"
        "v_fmac_f32 v221, v177, v162\n"
        "v_mfma_f32_16x16x32_bf16 v[192:195], v[124:127], v[232:235], v[192:195]\n"
        "v_fmac_f32 v222, v178, v162\n"
        "v_fmac_f32 v223, v179, v162\n"
        "v_mfma_f32_16x16x32_bf16 v[244:247], v[72:75], v[248:251], 0\n"
        "v_cvt_pk_bf16_f32 v188, v240, v241\n"
        "v_cvt_pk_bf16_f32 v189, v242, v243\n"
        "v_mfma_f32_16x16x32_bf16 v[168:171], v[84:87], v[248:251], 0\n"
        "v_cvt_pk_bf16_f32 v190, v220, v221\n"
        "v_cvt_pk_bf16_f32 v191, v222, v223\n"
        "v_mfma_f32_16x16x32_bf16 v[252:255], v[48:51], v[200:203], 0\n"
        "v_pk_max_i16 v188, v188, 0\n"
        "v_pk_max_i16 v189, v189, 0\n"
        "v_mfma_f32_16x16x32_bf16 v[184:187], v[56:59], v[200:203], 0\n"
        "v_pk_max_i16 v190, v190, 0\n"
        "v_pk_max_i16 v191, v191, 0\n"
        "v_mfma_f32_16x16x32_bf16 v[252:255], v[52:55], v[204:207], v[252:255]\n"
        "buffer_store_dwordx4 v[188:191], v154, s[44:47], 0 offen offset:2048 sc1\n"
        "v_max3_u32 v159, v159, v161, v162\n"
        "v_min3_u32 v160, v160, v161, v162\n"
        "v_mfma_f32_16x16x32_bf16 v[184:187], v[60:63], v[204:207], v[184:187]\n"
        "ds_read_b128 v[212:215], v152 offset:8192\n"
        "v_exp_f32 v244, v244\n"
        "ds_read_b128 v[232:235], v153 offset:8192\n"
        "v_mfma_f32_16x16x32_bf16 v[176:179], v[64:67], v[248:251], 0\n"
        "v_exp_f32 v245, v245\n"
        "v_mfma_f32_16x16x32_bf16 v[240:243], v[112:115], v[248:251], 0\n"
        "v_exp_f32 v246, v246\n"
        "v_mfma_f32_16x16x32_bf16 v[220:223], v[76:79], v[248:251], 0\n"
        "v_exp_f32 v247, v247\n"
        "v_exp_f32 v228, v228\n"
        "v_exp_f32 v196, v196\n"
        "v_cvt_pk_bf16_f32 v148, v228, 0\n"
        "v_exp_f32 v197, v197\n"
        "v_exp_f32 v198, v198\n"
        "v_exp_f32 v240, v240\n"
        "v_exp_f32 v199, v199\n"
        "v_cvt_pk_bf16_f32 v150, v240, 0\n"
        "v_exp_f32 v220, v220\n"
        "v_exp_f32 v221, v221\n"
        "s_waitcnt lgkmcnt(1)\n"
        "v_mfma_f32_16x16x32_bf16 v[200:203], v[32:35], v[212:215], 0\n"
        "v_cvt_pk_bf16_f32 v188, v244, v220\n"
        "v_cvt_pk_bf16_f32 v189, v245, v221\n"
        "v_mfma_f32_16x16x32_bf16 v[204:207], v[40:43], v[212:215], 0\n"
        "v_exp_f32 v222, v222\n"
        "s_waitcnt lgkmcnt(0)\n"
        "v_mfma_f32_16x16x32_bf16 v[200:203], v[36:39], v[232:235], v[200:203]\n"
        "v_cvt_pk_bf16_f32 v190, v246, v222\n"
        "v_exp_f32 v223, v223\n"
        "v_mfma_f32_16x16x32_bf16 v[248:251], v[48:51], v[212:215], 0\n"
        "v_cvt_pk_bf16_f32 v191, v247, v223\n"
        "v_exp_f32 v168, v168\n"
        "v_mfma_f32_16x16x32_bf16 v[204:207], v[44:47], v[232:235], v[204:207]\n"
        "v_cvt_pk_bf16_f32 v228, v196, v168\n"
        "v_exp_f32 v169, v169\n"
        "v_mfma_f32_16x16x32_bf16 v[240:243], v[56:59], v[212:215], 0\n"
        "v_cvt_pk_bf16_f32 v244, v200, v201\n"
        "v_cvt_pk_bf16_f32 v245, v202, v203\n"
        "v_mfma_f32_16x16x32_bf16 v[248:251], v[52:55], v[232:235], v[248:251]\n"
        "v_cvt_pk_bf16_f32 v229, v197, v169\n"
        "v_exp_f32 v170, v170\n"
        "v_mfma_f32_16x16x32_bf16 v[240:243], v[60:63], v[232:235], v[240:243]\n"
        "v_cvt_pk_bf16_f32 v246, v204, v205\n"
        "v_cvt_pk_bf16_f32 v247, v206, v207\n"
        "v_cvt_pk_bf16_f32 v230, v198, v170\n"
        "v_exp_f32 v171, v171\n"
        "v_mfma_f32_16x16x32_bf16 v[220:223], v[84:87], v[244:247], 0\n"
        "v_cvt_pk_bf16_f32 v231, v199, v171\n"
        "v_cvt_pk_bf16_f32 v212, v224, v208\n"
        "v_mfma_f32_16x16x32_bf16 v[200:203], v[80:83], v[244:247], 0\n"
        "v_cvt_pk_bf16_f32 v213, v225, v209\n"
        "v_cvt_pk_bf16_f32 v214, v226, v210\n"
        "v_mfma_f32_16x16x32_bf16 v[232:235], v[64:67], v[244:247], 0\n"
        "v_cvt_pk_bf16_f32 v215, v227, v211\n"
        "v_exp_f32 v176, v176\n"
        "v_mfma_f32_16x16x32_bf16 v[204:207], v[68:71], v[244:247], 0\n"
        "v_exp_f32 v220, v220\n"
        "v_mfma_f32_16x16x32_bf16 v[172:175], v[104:107], v[212:215], v[172:175]\n"
        "v_exp_f32 v200, v200\n"
        "v_mfma_f32_16x16x32_bf16 v[236:239], v[108:111], v[212:215], v[236:239]\n"
        "v_exp_f32 v201, v201\n"
        "v_mfma_f32_16x16x32_bf16 v[192:195], v[124:127], v[212:215], v[192:195]\n"
        "v_exp_f32 v202, v202\n"
        "v_mfma_f32_16x16x32_bf16 v[196:199], v[76:79], v[244:247], 0\n"
        "v_exp_f32 v203, v203\n"
        "v_mfma_f32_16x16x16_bf16 v[172:175], v[120:121], v[148:149], v[172:175]\n"
        "v_exp_f32 v221, v221\n"
        "v_exp_f32 v222, v222\n"
        "v_mfma_f32_16x16x16_bf16 v[192:195], v[124:125], v[148:149], v[192:195]\n"
        "v_exp_f32 v223, v223\n"
        "v_exp_f32 v177, v177\n"
        "v_mfma_f32_16x16x16_bf16 v[236:239], v[122:123], v[148:149], v[236:239]\n"
        "v_exp_f32 v178, v178\n"
        "v_exp_f32 v179, v179\n"
        "v_mfma_f32_16x16x32_bf16 v[168:171], v[72:75], v[244:247], 0\n"
        "v_exp_f32 v180, v180\n"
        "v_mfma_f32_16x16x32_bf16 v[224:227], v[112:115], v[244:247], 0\n"
        "v_cvt_pk_bf16_f32 v208, v176, v180\n"
        "v_rcp_f32 v161, v192\n"
        "s_setprio 0\n"
        "v_exp_f32 v181, v181\n"
        "v_fmac_f32 v164, v172, v161\n"
        "v_cvt_pk_bf16_f32 v209, v177, v181\n"
        "v_fmac_f32 v165, v173, v161\n"
        "v_fmac_f32 v166, v174, v161\n"
        "v_fmac_f32 v167, v175, v161\n"
        "v_fmac_f32 v216, v236, v161\n"
        "v_fmac_f32 v217, v237, v161\n"
        "v_fmac_f32 v218, v238, v161\n"
        "v_fmac_f32 v219, v239, v161\n"
        "v_cvt_pk_bf16_f32 v212, v164, v165\n"
        "v_cvt_pk_bf16_f32 v213, v166, v167\n"
        "v_cvt_pk_bf16_f32 v214, v216, v217\n"
        "v_cvt_pk_bf16_f32 v215, v218, v219\n"
        "v_pk_max_i16 v212, v212, 0\n"
        "v_pk_max_i16 v213, v213, 0\n"
        "v_pk_max_i16 v214, v214, 0\n"
        "v_pk_max_i16 v215, v215, 0\n"
        "v_exp_f32 v182, v182\n"
        "buffer_store_dwordx4 v[212:215], v154, s[44:47], s51 offen offset:0 sc1\n"
        "v_cvt_pk_bf16_f32 v210, v178, v182\n"
        "v_exp_f32 v183, v183\n"
        "v_exp_f32 v232, v232\n"
        "v_cvt_pk_bf16_f32 v211, v179, v183\n"
        "v_exp_f32 v233, v233\n"
        "v_exp_f32 v234, v234\n"
        "v_mfma_f32_16x16x32_bf16 v[244:247], v[88:91], v[208:211], 0\n"
        "s_mov_b32 m0, s61\n"
        "s_nop 0\n"
        "buffer_load_dwordx4 v155, s[40:43], s52 offen offset:0 nt lds\n"
        "v_exp_f32 v235, v235\n"
        "v_mfma_f32_16x16x32_bf16 v[192:195], v[92:95], v[208:211], 0\n"
        "v_exp_f32 v204, v204\n"
        "v_mfma_f32_16x16x32_bf16 v[172:175], v[124:127], v[208:211], 0\n"
        "ds_read_b128 v[236:239], v152 offset:10240\n"
        "v_cvt_pk_bf16_f32 v164, v232, v204\n"
        "v_exp_f32 v205, v205\n"
        "ds_read_b128 v[216:219], v153 offset:10240\n"
        "v_mfma_f32_16x16x32_bf16 v[244:247], v[96:99], v[188:191], v[244:247]\n"
        "v_cvt_pk_bf16_f32 v165, v233, v205\n"
        "v_exp_f32 v206, v206\n"
        "v_mfma_f32_16x16x32_bf16 v[192:195], v[100:103], v[188:191], v[192:195]\n"
        "v_cvt_pk_bf16_f32 v166, v234, v206\n"
        "v_exp_f32 v207, v207\n"
        "v_mfma_f32_16x16x32_bf16 v[172:175], v[124:127], v[188:191], v[172:175]\n"
        "v_cvt_pk_bf16_f32 v167, v235, v207\n"
        "v_cvt_pk_bf16_f32 v212, v200, v220\n"
        "v_mfma_f32_16x16x32_bf16 v[244:247], v[104:107], v[228:231], v[244:247]\n"
        "v_cvt_pk_bf16_f32 v213, v201, v221\n"
        "v_cvt_pk_bf16_f32 v214, v202, v222\n"
        "v_mfma_f32_16x16x32_bf16 v[192:195], v[108:111], v[228:231], v[192:195]\n"
        "v_cvt_pk_bf16_f32 v215, v203, v223\n"
        "v_exp_f32 v224, v224\n"
        "v_mfma_f32_16x16x32_bf16 v[172:175], v[124:127], v[228:231], v[172:175]\n"
        "v_cvt_pk_bf16_f32 v148, v224, 0\n"
        "v_exp_f32 v168, v168\n"
        "v_mfma_f32_16x16x16_bf16 v[244:247], v[120:121], v[150:151], v[244:247]\n"
        "v_exp_f32 v169, v169\n"
        "v_exp_f32 v170, v170\n"
        "s_waitcnt lgkmcnt(1)\n"
        "v_mfma_f32_16x16x32_bf16 v[176:179], v[32:35], v[236:239], 0\n"
        "v_exp_f32 v171, v171\n"
        "v_mfma_f32_16x16x32_bf16 v[180:183], v[40:43], v[236:239], 0\n"
        "v_exp_f32 v196, v196\n"
        "s_waitcnt lgkmcnt(0)\n"
        "v_mfma_f32_16x16x32_bf16 v[176:179], v[36:39], v[216:219], v[176:179]\n"
        "v_cvt_pk_bf16_f32 v208, v168, v196\n"
        "v_exp_f32 v197, v197\n"
        "v_mfma_f32_16x16x32_bf16 v[180:183], v[44:47], v[216:219], v[180:183]\n"
        "v_cvt_pk_bf16_f32 v209, v169, v197\n"
        "v_exp_f32 v198, v198\n"
        "v_mfma_f32_16x16x16_bf16 v[172:175], v[124:125], v[150:151], v[172:175]\n"
        "v_cvt_pk_bf16_f32 v210, v170, v198\n"
        "v_exp_f32 v199, v199\n"
        "v_mfma_f32_16x16x16_bf16 v[192:195], v[122:123], v[150:151], v[192:195]\n"
        "v_cvt_pk_bf16_f32 v188, v176, v177\n"
        "v_cvt_pk_bf16_f32 v189, v178, v179\n"
        "v_cvt_pk_bf16_f32 v190, v180, v181\n"
        "v_mfma_f32_16x16x32_bf16 v[232:235], v[88:91], v[164:167], 0\n"
        "v_cvt_pk_bf16_f32 v191, v182, v183\n"
        "v_rcp_f32 v162, v172\n"
        "v_mfma_f32_16x16x32_bf16 v[204:207], v[92:95], v[164:167], 0\n"
        "v_fmac_f32 v252, v244, v162\n"
        "v_fmac_f32 v253, v245, v162\n"
        "v_mfma_f32_16x16x32_bf16 v[200:203], v[64:67], v[188:191], 0\n"
        "v_fmac_f32 v254, v246, v162\n"
        "v_fmac_f32 v255, v247, v162\n"
        "v_mfma_f32_16x16x32_bf16 v[220:223], v[68:71], v[188:191], 0\n"
        "v_fmac_f32 v184, v192, v162\n"
        "v_fmac_f32 v185, v193, v162\n"
        "v_mfma_f32_16x16x32_bf16 v[228:231], v[124:127], v[164:167], 0\n"
        "v_fmac_f32 v186, v194, v162\n"
        "v_fmac_f32 v187, v195, v162\n"
        "v_mfma_f32_16x16x32_bf16 v[224:227], v[84:87], v[188:191], 0\n"
        "v_cvt_pk_bf16_f32 v176, v252, v253\n"
        "v_cvt_pk_bf16_f32 v177, v254, v255\n"
        "v_mfma_f32_16x16x32_bf16 v[180:183], v[76:79], v[188:191], 0\n"
        "v_cvt_pk_bf16_f32 v178, v184, v185\n"
        "v_cvt_pk_bf16_f32 v179, v186, v187\n"
        "v_mfma_f32_16x16x32_bf16 v[172:175], v[72:75], v[188:191], 0\n"
        "v_pk_max_i16 v176, v176, 0\n"
        "v_pk_max_i16 v177, v177, 0\n"
        "v_mfma_f32_16x16x32_bf16 v[244:247], v[48:51], v[236:239], 0\n"
        "v_pk_max_i16 v178, v178, 0\n"
        "v_pk_max_i16 v179, v179, 0\n"
        "v_mfma_f32_16x16x32_bf16 v[164:167], v[56:59], v[236:239], 0\n"
        "buffer_store_dwordx4 v[176:179], v154, s[44:47], s51 offen offset:2048 sc1\n"
        "v_max3_u32 v159, v159, v161, v162\n"
        "v_min3_u32 v160, v160, v161, v162\n"
        "v_mfma_f32_16x16x32_bf16 v[244:247], v[52:55], v[216:219], v[244:247]\n"
        "s_waitcnt vmcnt(9)\n"
        "s_barrier\n"
        "s_setprio 3\n"
        "v_exp_f32 v172, v172\n"
        "v_mfma_f32_16x16x32_bf16 v[164:167], v[60:63], v[216:219], v[164:167]\n"
        "ds_read_b128 v[192:195], v157 offset:64\n"
        "v_exp_f32 v173, v173\n"
        "ds_read_b128 v[184:187], v157 offset:128\n"
        "v_mfma_f32_16x16x32_bf16 v[236:239], v[112:115], v[188:191], 0\n"
        "ds_read_b128 v[176:179], v157 offset:0\n"
        "v_exp_f32 v174, v174\n"
        "v_mfma_f32_16x16x32_bf16 v[216:219], v[80:83], v[188:191], 0\n"
        "v_exp_f32 v175, v175\n"
        "v_exp_f32 v180, v180\n"
        "v_exp_f32 v181, v181\n"
        "v_exp_f32 v182, v182\n"
        "v_exp_f32 v183, v183\n"
        "v_cvt_pk_bf16_f32 v211, v171, v199\n"
        "v_exp_f32 v224, v224\n"
        "ds_read_b64 v[188:189], v157 offset:192\n"
        "v_mfma_f32_16x16x32_bf16 v[232:235], v[96:99], v[208:211], v[232:235]\n"
        "v_exp_f32 v216, v216\n"
        "v_mfma_f32_16x16x32_bf16 v[204:207], v[100:103], v[208:211], v[204:207]\n"
        "v_exp_f32 v217, v217\n"
        "v_mfma_f32_16x16x32_bf16 v[228:231], v[124:127], v[208:211], v[228:231]\n"
        "v_exp_f32 v218, v218\n"
        "v_mfma_f32_16x16x32_bf16 v[232:235], v[104:107], v[212:215], v[232:235]\n"
        "v_exp_f32 v219, v219\n"
        "v_mfma_f32_16x16x32_bf16 v[204:207], v[108:111], v[212:215], v[204:207]\n"
        "v_exp_f32 v225, v225\n"
        "v_mfma_f32_16x16x32_bf16 v[228:231], v[124:127], v[212:215], v[228:231]\n"
        "buffer_load_dwordx4 v155, s[40:43], s52 offen offset:2048 nt lds\n"
        "v_exp_f32 v226, v226\n"
        "s_waitcnt lgkmcnt(1)\n"
        "v_mfma_f32_16x16x32_bf16 v[136:139], v[88:91], v[176:179], v[136:139]\n"
        "v_exp_f32 v227, v227\n"
        "v_mfma_f32_16x16x32_bf16 v[136:139], v[96:99], v[192:195], v[136:139]\n"
        "v_exp_f32 v200, v200\n"
        "v_mfma_f32_16x16x32_bf16 v[136:139], v[104:107], v[184:187], v[136:139]\n"
        "v_cvt_pk_bf16_f32 v168, v172, v180\n"
        "v_exp_f32 v201, v201\n"
        "v_mfma_f32_16x16x16_bf16 v[228:231], v[124:125], v[148:149], v[228:231]\n"
        "v_cvt_pk_bf16_f32 v169, v173, v181\n"
        "v_exp_f32 v202, v202\n"
        "v_mfma_f32_16x16x16_bf16 v[232:235], v[120:121], v[148:149], v[232:235]\n"
        "v_cvt_pk_bf16_f32 v170, v174, v182\n"
        "v_exp_f32 v203, v203\n"
        "s_waitcnt lgkmcnt(0)\n"
        "v_mfma_f32_16x16x16_bf16 v[136:139], v[120:121], v[188:189], v[136:139]\n"
        "v_cvt_pk_bf16_f32 v171, v175, v183\n"
        "v_exp_f32 v220, v220\n"
        "v_mfma_f32_16x16x16_bf16 v[204:207], v[122:123], v[148:149], v[204:207]\n"
        "v_cvt_pk_bf16_f32 v196, v200, v220\n"
        "v_rcp_f32 v161, v228\n"
        "v_mfma_f32_16x16x32_bf16 v[140:143], v[92:95], v[176:179], v[140:143]\n"
        "v_fmac_f32 v248, v232, v161\n"
        "v_fmac_f32 v249, v233, v161\n"
        "v_mfma_f32_16x16x32_bf16 v[140:143], v[100:103], v[192:195], v[140:143]\n"
        "v_fmac_f32 v250, v234, v161\n"
        "v_fmac_f32 v251, v235, v161\n"
        "v_mfma_f32_16x16x32_bf16 v[140:143], v[108:111], v[184:187], v[140:143]\n"
        "v_exp_f32 v221, v221\n"
        "v_mfma_f32_16x16x32_bf16 v[144:147], v[124:127], v[176:179], v[144:147]\n"
        "v_cvt_pk_bf16_f32 v197, v201, v221\n"
        "v_fmac_f32 v240, v204, v161\n"
        "v_mfma_f32_16x16x32_bf16 v[144:147], v[124:127], v[192:195], v[144:147]\n"
        "v_exp_f32 v222, v222\n"
        "v_mfma_f32_16x16x32_bf16 v[144:147], v[124:127], v[184:187], v[144:147]\n"
        "v_cvt_pk_bf16_f32 v198, v202, v222\n"
        "v_fmac_f32 v241, v205, v161\n"
        "v_mfma_f32_16x16x16_bf16 v[140:143], v[122:123], v[188:189], v[140:143]\n"
        "v_exp_f32 v223, v223\n"
        "v_fmac_f32 v242, v206, v161\n"
        "v_cvt_pk_bf16_f32 v199, v203, v223\n"
        "v_mfma_f32_16x16x16_bf16 v[144:147], v[124:125], v[188:189], v[144:147]\n"
        "v_fmac_f32 v243, v207, v161\n"
        "v_cvt_pk_bf16_f32 v208, v248, v249\n"
        "v_cvt_pk_bf16_f32 v209, v250, v251\n"
        "v_mfma_f32_16x16x32_bf16 v[212:215], v[88:91], v[196:199], 0\n"
        "v_cvt_pk_bf16_f32 v210, v240, v241\n"
        "v_cvt_pk_bf16_f32 v211, v242, v243\n"
        "v_mfma_f32_16x16x32_bf16 v[172:175], v[92:95], v[196:199], 0\n"
        "v_pk_max_i16 v208, v208, 0\n"
        "v_pk_max_i16 v209, v209, 0\n"
        "v_mfma_f32_16x16x32_bf16 v[180:183], v[124:127], v[196:199], 0\n"
        "v_pk_max_i16 v210, v210, 0\n"
        "v_pk_max_i16 v211, v211, 0\n"
        "v_mfma_f32_16x16x32_bf16 v[212:215], v[96:99], v[168:171], v[212:215]\n"
        "buffer_store_dwordx4 v[208:211], v154, s[44:47], s52 offen offset:0 sc1\n"
        "v_cvt_pk_bf16_f32 v228, v216, v224\n"
        "v_cvt_pk_bf16_f32 v229, v217, v225\n"
        "v_mfma_f32_16x16x32_bf16 v[172:175], v[100:103], v[168:171], v[172:175]\n"
        "v_cvt_pk_bf16_f32 v230, v218, v226\n"
        "v_cvt_pk_bf16_f32 v231, v219, v227\n"
        "v_mfma_f32_16x16x32_bf16 v[180:183], v[124:127], v[168:171], v[180:183]\n"
        "v_exp_f32 v236, v236\n"
        "v_mfma_f32_16x16x32_bf16 v[212:215], v[104:107], v[228:231], v[212:215]\n"
        "v_cvt_pk_bf16_f32 v150, v236, 0\n"
        "v_add_u32 v152, s48, v152\n"
        "v_mfma_f32_16x16x32_bf16 v[172:175], v[108:111], v[228:231], v[172:175]\n"
        "ds_read_b128 v[248:251], v152 offset:0\n"
        "v_add_u32 v153, s48, v153\n"
        "v_mov_b32_dpp v116, v116 quad_perm:[1,2,3,0] row_mask:0xf bank_mask:0xf\n"
        "v_mfma_f32_16x16x32_bf16 v[180:183], v[124:127], v[228:231], v[180:183]\n"
        "ds_read_b128 v[252:255], v153 offset:0\n"
        "v_mov_b32_dpp v117, v117 quad_perm:[1,2,3,0] row_mask:0xf bank_mask:0xf\n"
        "v_mov_b32_dpp v118, v118 quad_perm:[1,2,3,0] row_mask:0xf bank_mask:0xf\n"
        "v_mfma_f32_16x16x16_bf16 v[212:215], v[120:121], v[150:151], v[212:215]\n"
        "v_mov_b32_dpp v119, v119 quad_perm:[1,2,3,0] row_mask:0xf bank_mask:0xf\n"
        "v_mov_b32_dpp v112, v112 quad_perm:[1,2,3,0] row_mask:0xf bank_mask:0xf\n"
        "v_mov_b32_dpp v113, v113 quad_perm:[1,2,3,0] row_mask:0xf bank_mask:0xf\n"
        "v_mfma_f32_16x16x16_bf16 v[180:183], v[124:125], v[150:151], v[180:183]\n"
        "v_mov_b32_dpp v114, v114 quad_perm:[1,2,3,0] row_mask:0xf bank_mask:0xf\n"
        "v_mov_b32_dpp v115, v115 quad_perm:[1,2,3,0] row_mask:0xf bank_mask:0xf\n"
        "v_mov_b32_dpp v157, v157 quad_perm:[3,0,1,2] row_mask:0xf bank_mask:0xf\n"
        "v_mfma_f32_16x16x16_bf16 v[172:175], v[122:123], v[150:151], v[172:175]\n"
        "v_alignbit_b32 v120, v121, v120, 16\n"
        "v_lshrrev_b32 v121, 16, v121\n"
        "v_alignbit_b32 v122, v123, v122, 16\n"
        "v_lshrrev_b32 v123, 16, v123\n"
        "v_rcp_f32 v162, v180\n"
        "s_nop 0\n"
        "v_fmac_f32 v244, v212, v162\n"
        "v_fmac_f32 v245, v213, v162\n"
        "v_fmac_f32 v246, v214, v162\n"
        "v_fmac_f32 v247, v215, v162\n"
        "v_fmac_f32 v164, v172, v162\n"
        "v_fmac_f32 v165, v173, v162\n"
        "v_fmac_f32 v166, v174, v162\n"
        "v_fmac_f32 v167, v175, v162\n"
        "v_cvt_pk_bf16_f32 v232, v244, v245\n"
        "v_cvt_pk_bf16_f32 v233, v246, v247\n"
        "v_cvt_pk_bf16_f32 v234, v164, v165\n"
        "v_cvt_pk_bf16_f32 v235, v166, v167\n"
        "v_pk_max_i16 v232, v232, 0\n"
        "v_pk_max_i16 v233, v233, 0\n"
        "v_pk_max_i16 v234, v234, 0\n"
        "s_setprio 2\n"
        "v_pk_max_i16 v235, v235, 0\n"
        "v_max3_u32 v159, v159, v161, v162\n"
        "v_min3_u32 v160, v160, v161, v162\n"
        "buffer_store_dwordx4 v[232:235], v154, s[44:47], s52 offen offset:2048 sc1\n"
        "s_add_u32 s44, s44, s36\n"
        "s_addc_u32 s45, s45, 0\n"
        "s_add_u32 s49, s49, 1\n"
        "s_cmp_lt_u32 s49, 4\n"
        "s_cbranch_scc1 1b\n"
        "s_waitcnt lgkmcnt(0)\n"
        "s_nop 4\n"
        "v_rcp_f32 v164, v144\n"
        "s_mov_b32 s58, 0x5f800000\n"
        "v_fmac_f32 v128, v136, v164\n"
        "v_fmac_f32 v129, v137, v164\n"
        "v_fmac_f32 v130, v138, v164\n"
        "v_fmac_f32 v131, v139, v164\n"
        "v_fmac_f32 v132, v140, v164\n"
        "v_fmac_f32 v133, v141, v164\n"
        "v_fmac_f32 v134, v142, v164\n"
        "v_fmac_f32 v135, v143, v164\n"
        "v_cvt_pk_bf16_f32 v168, v128, v129\n"
        "v_cvt_pk_bf16_f32 v169, v130, v131\n"
        "v_cvt_pk_bf16_f32 v170, v132, v133\n"
        "v_cvt_pk_bf16_f32 v171, v134, v135\n"
        "v_pk_max_i16 v168, v168, 0\n"
        "v_pk_max_i16 v169, v169, 0\n"
        "v_pk_max_i16 v170, v170, 0\n"
        "v_pk_max_i16 v171, v171, 0\n"
        "v_max_u32 v159, v159, v164\n"
        "v_min_u32 v160, v160, v164\n"
        "v_add_f32 v165, v128, v129\n"
        "v_add_f32 v165, v165, v130\n"
        "v_add_f32 v165, v165, v131\n"
        "v_add_f32 v165, v165, v132\n"
        "v_add_f32 v165, v165, v133\n"
        "v_add_f32 v165, v165, v134\n"
        "v_add_f32 v165, v165, v135\n"
        "v_cmp_u_f32 s[56:57], v165, v165\n"
        "v_cmp_lt_u32 vcc, s58, v159\n"
        "global_store_dwordx4 v158, v[168:171], s[38:39] sc1\n"
        "s_mov_b32 s59, 0x3c000000\n"
        "v_cmp_gt_u32 s[54:55], s59, v160\n"
        "s_or_b64 s[56:57], s[56:57], vcc\n"
        "s_nop 0\n"
        "s_or_b64 s[56:57], s[56:57], s[54:55]\n"
        "s_or_b32 %[flag], s56, s57\n"
        "s_mov_b32 m0, s68\n"
        "s_branch 99f\n"
        "13:\n"
        "s_mov_b32 m0, s50\n"
        "s_nop 0\n"
        "buffer_load_dwordx4 v155, s[40:43], 0 offen offset:0 nt lds\n"
        "buffer_load_dwordx4 v155, s[40:43], 0 offen offset:2048 nt lds\n"
        "s_add_u32 m0, s50, 4096\n"
        "s_nop 0\n"
        "buffer_load_dwordx4 v155, s[40:43], s51 offen offset:0 nt lds\n"
        "buffer_load_dwordx4 v155, s[40:43], s51 offen offset:2048 nt lds\n"
        "s_add_u32 m0, s50, 8192\n"
        "s_nop 0\n"
        "buffer_load_dwordx4 v155, s[40:43], s52 offen offset:0 nt lds\n"
        "buffer_load_dwordx4 v155, s[40:43], s52 offen offset:2048 nt lds\n"
        "s_add_u32 s40, s40, s36\n"
        "s_addc_u32 s41, s41, 0\n"
        "s_add_u32 s50, s50, 49152\n"
        "s_waitcnt lgkmcnt(0)\n"
        "s_barrier\n"
        "s_branch 6b\n"
        "8:\n"
        "s_waitcnt vmcnt(3)\n"
        "s_sub_u32 s40, s40, 25600\n"
        "s_subb_u32 s41, s41, 0\n"
        "s_sub_u32 s50, s50, 24576\n"
        "s_mov_b32 m0, s50\n"
        "s_nop 0\n"
        "buffer_load_dwordx4 v155, s[40:43], 0 offen offset:0 nt lds\n"
        "buffer_load_dwordx4 v155, s[40:43], 0 offen offset:2048 nt lds\n"
        "s_add_u32 m0, s50, 4096\n"
        "s_nop 0\n"
        "buffer_load_dwordx4 v155, s[40:43], s51 offen offset:0 nt lds\n"
        "buffer_load_dwordx4 v155, s[40:43], s51 offen offset:2048 nt lds\n"
        "s_add_u32 m0, s50, 8192\n"
        "s_nop 0\n"
        "buffer_load_dwordx4 v155, s[40:43], s52 offen offset:0 nt lds\n"
        "buffer_load_dwordx4 v155, s[40:43], s52 offen offset:2048 nt lds\n"
        "s_add_u32 s40, s40, 25600\n"
        "s_addc_u32 s41, s41, 0\n"
        "s_add_u32 s50, s50, 24576\n"
        "s_mov_b32 m0, s50\n"
        "s_nop 0\n"
        "buffer_load_dwordx4 v155, s[40:43], 0 offen offset:0 nt lds\n"
        "buffer_load_dwordx4 v155, s[40:43], 0 offen offset:2048 nt lds\n"
        "s_add_u32 m0, s50, 4096\n"
        "s_nop 0\n"
        "buffer_load_dwordx4 v155, s[40:43], s51 offen offset:0 nt lds\n"
        "buffer_load_dwordx4 v155, s[40:43], s51 offen offset:2048 nt lds\n"
        "s_add_u32 m0, s50, 8192\n"
        "s_nop 0\n"
        "buffer_load_dwordx4 v155, s[40:43], s52 offen offset:0 nt lds\n"
        "buffer_load_dwordx4 v155, s[40:43], s52 offen offset:2048 nt lds\n"
        "s_waitcnt vmcnt(12)\n"
        "ds_write_b32 v171, v160 offset:1596\n"
        "s_mul_i32 s56, s35, 3200\n"
        "s_sub_u32 s56, 1600, s56\n"
        "v_add_u32 v171, s56, v171\n"
        "11:\n"
        "ds_read_b32 v164, v171 offset:1596\n"
        "s_waitcnt lgkmcnt(0)\n"
        "v_readfirstlane_b32 s56, v164\n"
        "s_nop 3\n"
        "s_cmp_eq_u32 s56, 0\n"
        "s_cbranch_scc0 12b\n"
        "s_sleep 1\n"
        "s_branch 11b\n"
        "99:\n"
        : [flag] "=&s"(flag)
        : [ka] "s"(__builtin_amdgcn_kernarg_segment_ptr()), [w] "s"(blockIdx.x), [j] "s"(j), [lds] "s"((unsigned)(uintptr_t)lds)
        : "v0", "v1", "v2", "v3", "v4", "v5", "v6", "v7", "v8", "v9", "v10", "v11", "v12", "v13", "v14", "v15", "v16", "v17", "v18", "v19", "v20", "v21", "v22", "v23", "v24", "v25", "v26", "v27", "v28", "v29", "v30", "v31", "v32", "v33", "v34", "v35", "v36", "v37", "v38", "v39", "v40", "v41", "v42", "v43", "v44", "v45", "v46", "v47", "v48", "v49", "v50", "v51", "v52", "v53", "v54", "v55", "v56", "v57", "v58", "v59", "v60", "v61", "v62", "v63", "v64", "v65", "v66", "v67", "v68", "v69", "v70", "v71", "v72", "v73", "v74", "v75", "v76", "v77", "v78", "v79", "v80", "v81", "v82", "v83", "v84", "v85", "v86", "v87", "v88", "v89", "v90", "v91", "v92", "v93", "v94", "v95", "v96", "v97", "v98", "v99", "v100", "v101", "v102", "v103", "v104", "v105", "v106", "v107", "v108", "v109", "v110", "v111", "v112", "v113", "v114", "v115", "v116", "v117", "v118", "v119", "v120", "v121", "v122", "v123", "v124", "v125", "v126", "v127", "v128", "v129", "v130", "v131", "v132", "v133", "v134", "v135", "v136", "v137", "v138", "v139", "v140", "v141", "v142", "v143", "v144", "v145", "v146", "v147", "v148", "v149", "v150", "v151", "v152", "v153", "v154", "v155", "v156", "v157", "v158", "v159", "v160", "v161", "v162", "v163", "v164", "v165", "v166", "v167", "v168", "v169", "v170", "v171", "v172", "v173", "v174", "v175", "v176", "v177", "v178", "v179", "v180", "v181", "v182", "v183", "v184", "v185", "v186", "v187", "v188", "v189", "v190", "v191", "v192", "v193", "v194", "v195", "v196", "v197", "v198", "v199", "v200", "v201", "v202", "v203", "v204", "v205", "v206", "v207", "v208", "v209", "v210", "v211", "v212", "v213", "v214", "v215", "v216", "v217", "v218", "v219", "v220", "v221", "v222", "v223", "v224", "v225", "v226", "v227", "v228", "v229", "v230", "v231", "v232", "v233", "v234", "v235", "v236", "v237", "v238", "v239", "v240", "v241", "v242", "v243", "v244", "v245", "v246", "v247", "v248", "v249", "v250", "v251", "v252", "v253", "v254", "v255", "s34", "s35", "s36", "s37", "s38", "s39", "s40", "s41", "s42", "s43", "s44", "s45", "s46", "s47", "s48", "s49", "s50", "s51", "s52", "s53", "s54", "s55", "s56", "s57", "s58", "s59", "s60", "s61", "s62", "s63", "s64", "s65", "s66", "s67", "s68", "s69", "vcc", "scc", "memory");
    if (__builtin_expect(flag != 0, 0)) {
        asm volatile("s_waitcnt vmcnt(0)" ::: "memory");
        const int lane = __builtin_amdgcn_mbcnt_hi(~0u, __builtin_amdgcn_mbcnt_lo(~0u, 0u)), c = lane & 15, g = lane >> 4, h = j & 1;
        const size_t b0 = ((size_t)blockIdx.x * 4 + (j >> 1)) * BATB;
        Wfrag W;
        load_all_w(W, wq, wk, wv, wres, h, c, g);
#pragma unroll 1
        for (int u = 0; u < 4; ++u) slow_unit(W, e + b0 + (size_t)u * stride, out + b0 + h * 64 + (size_t)u * stride, c, g);
    }
}

__global__ void __launch_bounds__(128, 2)
autoint_generic(const char* __restrict__ e, const char* __restrict__ wq, const char* __restrict__ wk, const char* __restrict__ wv,
                const char* __restrict__ wres, char* __restrict__ out) {
    const int lane = threadIdx.x & 63, c = lane & 15, g = lane >> 4, h = threadIdx.x >> 6;
    Wfrag W;
    load_all_w(W, wq, wk, wv, wres, h, c, g);
    slow_unit(W, e + (size_t)blockIdx.x * BATB, out + (size_t)blockIdx.x * BATB + h * 64, c, g);
}

extern "C" void kernel_launch(void* const* d_in, const int* in_sizes, int n_in, void* d_out, int out_size, void* d_ws, size_t ws_size,
                              hipStream_t stream) {
    const int nb = in_sizes[0] / (NF * ND);
    if (nb % 16 == 0)
        autoint_kernel<<<nb / 16, 512, 0, stream>>>((const char*)d_in[0], (char*)d_out, (unsigned)(nb / 4) * BATB, 0x27800000u  ,
                                                    (const char*)d_in[2], (const char*)d_in[3], (const char*)d_in[1], (const char*)d_in[4]);
    else
        autoint_generic<<<nb, 128, 0, stream>>>((const char*)d_in[0], (const char*)d_in[1], (const char*)d_in[2], (const char*)d_in[3],
                                                (const char*)d_in[4], (char*)d_out);
}
```
